```python
import torch

def _softmax(x, axis=-1):
    return torch.softmax(x, dim=axis)
def _gelu(x):
    return torch.nn.functional.gelu(x, approximate="tanh")
def _silu(x):
    return torch.nn.functional.silu(x)
def _sigmoid(x):
    return torch.sigmoid(x)
def _layernorm(x, axis=-1, eps=1e-5):
    m = x.mean(dim=axis, keepdim=True)
    v = x.var(dim=axis, keepdim=True, unbiased=False)
    return (x - m) / torch.sqrt(v + eps)
def _scatter_add(tgt, idx, src, axis=0):
    return tgt.clone().index_add_(axis, idx, src)
def _segment_sum(src, seg, n):
    out = torch.zeros((n,) + src.shape[1:], dtype=src.dtype)
    return out.index_add_(0, seg, src)
def _pad(x, pw):
    flat = tuple(v for pair in reversed(pw) for v in pair)
    return torch.nn.functional.pad(x, flat)

kernel_name = 'sequential_preLN__mha_block_cached__glu_gated_glu__decode'

def setup_inputs():

    _w = (lambda a: (lambda t: t.to(torch.bfloat16) if t.is_floating_point() else t)(torch.as_tensor(a)))
    def _A_setup():
        B, Sq, D = 1, 1, 640
        H, Sk, Dh = 32, 32768, 192
        return {
            "x": _w((lambda *s: torch.randn(*s, dtype=torch.bfloat16) * 0.05)(B, Sq, D)),
            "k_cache": _w((lambda *s: torch.randn(*s, dtype=torch.bfloat16) * 3.2)(B, H, Sk, Dh)),
            "v_cache": _w((lambda *s: torch.randn(*s, dtype=torch.bfloat16) * 0.4)(B, H, Sk, Dh)),
            "wq": _w((lambda *s: torch.randn(*s, dtype=torch.bfloat16) * 3.2)(D, H * Dh) * (D ** -0.5)),
            "wo": _w((lambda *s: torch.randn(*s, dtype=torch.bfloat16) * 0.4)(H * Dh, D) * ((H * Dh) ** -0.5)),
        }
    _A_in = _A_setup()

    def _B_setup():
        B, S, D, F = 1, 1, 640, 1536
        return {
            "x": _w((lambda *s: torch.randn(*s, dtype=torch.bfloat16) * 0.05)(B, S, D)),
            "w_gate": _w((lambda *s: torch.randn(*s, dtype=torch.bfloat16) * 0.1)(D, F)),
            "w_up": _w((lambda *s: torch.randn(*s, dtype=torch.bfloat16) * 0.05)(D, F)),
            "w_down": _w((lambda *s: torch.randn(*s, dtype=torch.bfloat16) * 0.05)(F, D)),
        }
    _B_in = _B_setup()

    return {
        "A_x": _A_in["x"],
        "A_k_cache": _A_in["k_cache"],
        "A_v_cache": _A_in["v_cache"],
        "A_wq": _A_in["wq"],
        "A_wo": _A_in["wo"],
        "B_x": _B_in["x"],
        "B_w_gate": _B_in["w_gate"],
        "B_w_up": _B_in["w_up"],
        "B_w_down": _B_in["w_down"],
    }

def reference(A_x, A_k_cache, A_v_cache, A_wq, A_wo, B_x, B_w_gate, B_w_up, B_w_down):
    def _A_ref(x, k_cache, v_cache, wq, wo):
        B, Sq, D = x.shape
        H, Dh = 32, 192
        q = (lambda x, *d: x.permute(*d))((x @ wq).reshape(B, Sq, H, Dh), 0, 2, 1, 3)
        scores = torch.einsum("bhqd,bhkd->bhqk", q, k_cache) * (Dh ** -0.5)
        p = _softmax(scores, axis=-1)
        o = torch.einsum("bhqk,bhkd->bhqd", p, v_cache)
        return (lambda x, *d: x.permute(*d))(o, 0, 2, 1, 3).reshape(B, Sq, H * Dh) @ wo

    def _B_ref(x, w_gate, w_up, w_down):
        g = torch.einsum("bsd,df->bsf", x, w_gate)
        u = torch.einsum("bsd,df->bsf", x, w_up)
        h = _sigmoid(g) * u
        return torch.einsum("bsf,fd->bsd", h, w_down)

    x = A_x
    h = x + _A_ref(_layernorm(x, axis=-1), A_k_cache, A_v_cache, A_wq, A_wo)
    h2 = h + _B_ref(_layernorm(h, axis=-1), B_w_gate, B_w_up, B_w_down)
    return h2
```

```cpp
#include <hip/hip_runtime.h>
#include <hip/hip_bf16.h>
#include <stdint.h>
#define MIMIC 1
typedef unsigned short u16;
typedef unsigned u32x4 __attribute__((ext_vector_type(4)));
typedef __bf16 bf16x2 __attribute__((ext_vector_type(2)));
constexpr int D = 640, H = 32, SK = 32768, DH = 192, F = 1536, HD = H * DH;
constexpr int NSPLIT = 16;
__device__ __forceinline__ float bf2f(u16 x) { return __uint_as_float(((unsigned)x) << 16); }
__device__ __forceinline__ u16 f2bf(float f) { unsigned u = __float_as_uint(f); return (u16)((u + 0x7fffu + ((u >> 16) & 1u)) >> 16); }
__device__ __forceinline__ float rbf(float f) { return MIMIC ? bf2f(f2bf(f)) : f; }
__device__ __forceinline__ float dot2(unsigned k, unsigned q, float acc) {
  return __builtin_amdgcn_fdot2_f32_bf16(__builtin_bit_cast(bf16x2, k), __builtin_bit_cast(bf16x2, q), acc, false);
}
template <int CTRL> __device__ __forceinline__ float dpp(float x) {
  return __builtin_bit_cast(float, __builtin_amdgcn_mov_dpp(__builtin_bit_cast(int, x), CTRL, 0xf, 0xf, true));
}
__device__ float block_sum(float v, float* red) {
  for (int o = 32; o > 0; o >>= 1) v += __shfl_xor(v, o);
  __syncthreads();
  if ((threadIdx.x & 63) == 0) red[threadIdx.x >> 6] = v;
  __syncthreads();
  float s = 0; for (int i = 0; i < (int)(blockDim.x >> 6); ++i) s += red[i];
  return s;
}
__device__ void ln640(const float* in, float* out, float* red) {
  float s = 0; for (int i = threadIdx.x; i < D; i += blockDim.x) s += in[i];
  float m = rbf(block_sum(s, red) / D);
  float mt = block_sum(s, red) / D;
  float v = 0; for (int i = threadIdx.x; i < D; i += blockDim.x) { float d = in[i] - mt; v += d * d; }
  v = rbf(block_sum(v, red) / D);
  float sd = rbf(sqrtf(rbf(v + 1e-5f)));
  for (int i = threadIdx.x; i < D; i += blockDim.x) out[i] = rbf(rbf(in[i] - m) / sd);
  __syncthreads();
}
__global__ void k_q(const u16* x, const u16* wq, u16* q) {
  __shared__ float xs[D], xn[D], red[8], part[4][64];
  for (int i = threadIdx.x; i < D; i += 256) xs[i] = bf2f(x[i]);
  __syncthreads();
  ln640(xs, xn, red);
  int c = blockIdx.x * 64 + (threadIdx.x & 63), kq = threadIdx.x >> 6;
  float a = 0;
  for (int k = kq * 160; k < kq * 160 + 160; ++k) a += xn[k] * bf2f(wq[(size_t)k * HD + c]);
  part[kq][threadIdx.x & 63] = a;
  __syncthreads();
  if (threadIdx.x < 64) q[c] = f2bf(part[0][threadIdx.x] + part[1][threadIdx.x] + part[2][threadIdx.x] + part[3][threadIdx.x]);
}
__global__ void __launch_bounds__(256) k_attn(const u16* q, const u16* K, const u16* V, float* pm, float* po) {
  __shared__ float sm[4][8][2]; __shared__ float so[32][DH];
  const int h = blockIdx.x / NSPLIT, s = blockIdx.x % NSPLIT;
  const int lane = threadIdx.x & 63, wave = threadIdx.x >> 6, r = lane >> 3, j = lane & 7;
  u32x4 qv[3];
  for (int i = 0; i < 3; ++i) qv[i] = *(const u32x4*)(q + h * DH + (j + 8 * i) * 8);
  constexpr int KPS = SK / NSPLIT, G = 4;
  const size_t base = ((size_t)h * SK + (size_t)s * KPS) * DH;
  float m = -INFINITY, l = 0, acc[24];
  for (int i = 0; i < 24; ++i) acc[i] = 0;
  const float scale = 0.07216878364870322f, L2E = 1.4426950408889634f;
  for (int t = wave * 8 * G; t < KPS; t += 4 * 8 * G) {
    u32x4 kv[G][3], vv[G][3];
#pragma unroll
    for (int g = 0; g < G; ++g)
#pragma unroll
      for (int i = 0; i < 3; ++i) kv[g][i] = __builtin_nontemporal_load((const u32x4*)(K + base + (size_t)(t + g * 8 + r) * DH + (j + 8 * i) * 8));
#pragma unroll
    for (int g = 0; g < G; ++g)
#pragma unroll
      for (int i = 0; i < 3; ++i) vv[g][i] = __builtin_nontemporal_load((const u32x4*)(V + base + (size_t)(t + g * 8 + r) * DH + (j + 8 * i) * 8));
    float sc[G], mn = m;
#pragma unroll
    for (int g = 0; g < G; ++g) {
      float d = 0;
#pragma unroll
      for (int i = 0; i < 3; ++i)
#pragma unroll
        for (int e = 0; e < 4; ++e) d = dot2(kv[g][i][e], qv[i][e], d);
      d += dpp<0xB1>(d); d += dpp<0x4E>(d); d += dpp<0x141>(d);
      sc[g] = rbf(rbf(d) * scale) * L2E; mn = fmaxf(mn, sc[g]);
    }
    float al = __builtin_amdgcn_exp2f(m - mn); m = mn; l *= al;
#pragma unroll
    for (int i = 0; i < 24; ++i) acc[i] *= al;
#pragma unroll
    for (int g = 0; g < G; ++g) {
      float p = __builtin_amdgcn_exp2f(sc[g] - mn); l += p;
#pragma unroll
      for (int i = 0; i < 3; ++i)
#pragma unroll
        for (int e = 0; e < 4; ++e) {
          acc[i * 8 + 2 * e] += p * __uint_as_float(vv[g][i][e] << 16);
          acc[i * 8 + 2 * e + 1] += p * __uint_as_float(vv[g][i][e] & 0xffff0000u);
        }
    }
  }
  if (j == 0) { sm[wave][r][0] = m; sm[wave][r][1] = l; }
  __syncthreads();
  float M = -INFINITY;
  for (int w = 0; w < 4; ++w) for (int rr = 0; rr < 8; ++rr) M = fmaxf(M, sm[w][rr][0]);
  float w_ = __builtin_amdgcn_exp2f(m - M);
  for (int i = 0; i < 3; ++i) for (int e = 0; e < 8; ++e) so[wave * 8 + r][(j + 8 * i) * 8 + e] = acc[i * 8 + e] * w_;
  __syncthreads();
  if (threadIdx.x < DH) {
    float o = 0; for (int k = 0; k < 32; ++k) o += so[k][threadIdx.x];
    po[(size_t)blockIdx.x * DH + threadIdx.x] = o;
  }
  if (threadIdx.x == 0) {
    float L = 0; for (int w = 0; w < 4; ++w) for (int rr = 0; rr < 8; ++rr) L += sm[w][rr][1] * __builtin_amdgcn_exp2f(sm[w][rr][0] - M);
    pm[blockIdx.x * 2] = M; pm[blockIdx.x * 2 + 1] = L;
  }
}
__global__ void k_wo(const float* pm, const float* po, const u16* wo, float* y) {
  __shared__ float o[DH];
  const int h = blockIdx.x / 5, cb = blockIdx.x % 5;
  float M = -INFINITY; for (int s = 0; s < NSPLIT; ++s) M = fmaxf(M, pm[(h * NSPLIT + s) * 2]);
  float L = 0; for (int s = 0; s < NSPLIT; ++s) L += pm[(h * NSPLIT + s) * 2 + 1] * __builtin_amdgcn_exp2f(pm[(h * NSPLIT + s) * 2] - M);
  for (int d = threadIdx.x; d < DH; d += 128) {
    float a = 0; for (int s = 0; s < NSPLIT; ++s) a += po[(size_t)(h * NSPLIT + s) * DH + d] * __builtin_amdgcn_exp2f(pm[(h * NSPLIT + s) * 2] - M);
    o[d] = rbf(a / L);
  }
  __syncthreads();
  int c = cb * 128 + threadIdx.x; float a = 0;
  for (int d = 0; d < DH; ++d) a += o[d] * bf2f(wo[(size_t)(h * DH + d) * D + c]);
  y[h * D + c] = a;
}
__global__ void k_ff1(const u16* x, const float* y, const u16* wg, const u16* wu, float* hbuf, float* ff) {
  __shared__ float hs[D], hn[D], red[8], pg[4][64], pu[4][64];
  for (int i = threadIdx.x; i < D; i += 256) { float a = 0; for (int h = 0; h < H; ++h) a += y[h * D + i]; hs[i] = rbf(bf2f(x[i]) + rbf(a)); if (blockIdx.x == 0) hbuf[i] = hs[i]; }
  __syncthreads();
  ln640(hs, hn, red);
  int c = blockIdx.x * 64 + (threadIdx.x & 63), kq = threadIdx.x >> 6;
  float g = 0, u = 0;
  for (int k = kq * 160; k < kq * 160 + 160; ++k) { g += hn[k] * bf2f(wg[(size_t)k * F + c]); u += hn[k] * bf2f(wu[(size_t)k * F + c]); }
  pg[kq][threadIdx.x & 63] = g; pu[kq][threadIdx.x & 63] = u;
  __syncthreads();
  if (threadIdx.x < 64) {
    int t = threadIdx.x;
    float gg = rbf(pg[0][t] + pg[1][t] + pg[2][t] + pg[3][t]), uu = rbf(pu[0][t] + pu[1][t] + pu[2][t] + pu[3][t]);
    float sg = rbf(1.f / (1.f + __expf(-gg)));
    ff[c] = rbf(sg * uu);
  }
}
__global__ void k_ff2(const float* hbuf, const float* ff, const u16* wd, u16* out) {
  __shared__ float p[16][64];
  int c = blockIdx.x * 64 + (threadIdx.x & 63), kq = threadIdx.x >> 6;
  float a = 0;
  for (int k = kq * 96; k < kq * 96 + 96; ++k) a += ff[k] * bf2f(wd[(size_t)k * D + c]);
  p[kq][threadIdx.x & 63] = a;
  __syncthreads();
  if (threadIdx.x < 64) { float s = 0; for (int i = 0; i < 16; ++i) s += p[i][threadIdx.x]; out[c] = f2bf(hbuf[c] + rbf(s)); }
}
extern "C" void kernel_launch(void* const* d_in, const int* in_sizes, int n_in, void* d_out, int out_size, void* d_ws, size_t ws_size, hipStream_t stream) {
  const u16* x = (const u16*)d_in[0]; const u16* K = (const u16*)d_in[1]; const u16* V = (const u16*)d_in[2];
  const u16* wq = (const u16*)d_in[3]; const u16* wo = (const u16*)d_in[4];
  const u16* wg = (const u16*)d_in[6]; const u16* wu = (const u16*)d_in[7]; const u16* wd = (const u16*)d_in[8];
  char* ws = (char*)d_ws;
  u16* q = (u16*)ws; float* pm = (float*)(ws + 65536); float* po = (float*)(ws + 131072); float* y = (float*)(ws + 1048576);
  float* hbuf = (float*)(ws + 2097152); float* ff = (float*)(ws + 2097152 + 65536);
  k_q<<<96, 256, 0, stream>>>(x, wq, q);
  k_attn<<<H * NSPLIT, 256, 0, stream>>>(q, K, V, pm, po);
  k_wo<<<H * 5, 128, 0, stream>>>(pm, po, wo, y);
  k_ff1<<<24, 256, 0, stream>>>(x, y, wg, wu, hbuf, ff);
  k_ff2<<<10, 1024, 0, stream>>>(hbuf, ff, wd, (u16*)d_out);
}
```

```cpp
#include <hip/hip_runtime.h>
#include <hip/hip_bf16.h>
#include <stdint.h>

#define STAMPS 0
#define PACE 0
#define PACE_BURST 100

typedef unsigned u32;
typedef unsigned long long u64;
typedef unsigned short u16;
typedef u32 u32x2 __attribute__((ext_vector_type(2)));
typedef u32 u32x3 __attribute__((ext_vector_type(3)));
typedef u32 u32x4 __attribute__((ext_vector_type(4)));
typedef float f32x2 __attribute__((ext_vector_type(2)));
typedef float f32x4 __attribute__((ext_vector_type(4)));
typedef __bf16 bf16x2 __attribute__((ext_vector_type(2)));
typedef __amdgpu_buffer_rsrc_t rsrc_t;
#define LAS __attribute__((address_space(3)))

namespace {
constexpr int D = 640, H = 32, SK = 32768, DH = 192, F = 1536;
constexpr u32 HEADB = (u32)SK * DH * 2;
constexpr int NTHR = 320, SVC = 4;
constexpr u32 OOB = 0x80000000u;
constexpr int AUX_NT = 2, AUX_SC1 = 16;
constexpr u32 WS_E = 0, WS_X0 = 4096, WS_X1 = 32768, WS_X3 = 49152, WS_X4 = 90112, WS_X2 = 131072, WS_BYTES = 2097152;
constexpr u32 WS_STAMP = 2097152;
constexpr int L_Q = 0, L_QP = 1536, L_FLAG = 2048, L_ST = 2176, L_GU = 2240, L_DN = 2560, L_OBF = 2688, L_H = 3840, L_RED = 5120,
              L_S = 8192, L_PART = 16384, L_WO = 28672, L_END = 59392, LDS_BYTES = 84 * 1024;
constexpr int F_K = 0, F_P = 4, F_V = 8, F_O = 12, F_DEAD = 13;
constexpr u32 SPIN_G = 1u << 17, SPIN_L = 1u << 21;

struct Args {
  const u16 *x, *k, *v, *wq, *wo, *wg, *wu, *wd;
  u16* out;
  unsigned char* ws;
};

__device__ __forceinline__ float dot2(u32 a, u32 b, float c) {
  return __builtin_amdgcn_fdot2_f32_bf16(__builtin_bit_cast(bf16x2, a), __builtin_bit_cast(bf16x2, b), c, false);
}
__device__ __forceinline__ u32 pk_bf16(float lo, float hi) {
  bf16x2 r = {(__bf16)lo, (__bf16)hi};
  return __builtin_bit_cast(u32, r);
}
__device__ __forceinline__ float lo_f(u32 a) { return __uint_as_float(a << 16); }
__device__ __forceinline__ float hi_f(u32 a) { return __uint_as_float(a & 0xffff0000u); }
__device__ __forceinline__ float rbf(float a) { return lo_f(pk_bf16(a, a)); }
__device__ __forceinline__ u32 zip_lo(u32 a, u32 b) { return __builtin_amdgcn_perm(b, a, 0x05040100u); }
__device__ __forceinline__ u32 zip_hi(u32 a, u32 b) { return __builtin_amdgcn_perm(b, a, 0x07060302u); }
template <int CTRL> __device__ __forceinline__ float dpp(float x) {
  return __builtin_bit_cast(float, __builtin_amdgcn_mov_dpp(__builtin_bit_cast(int, x), CTRL, 0xf, 0xf, true));
}
constexpr int X1 = 0xB1, X2 = 0x4E, X7 = 0x141, R1 = 0x121, R2 = 0x122, R4 = 0x124, R8 = 0x128;
__device__ __forceinline__ float tsum32(float a, float b) {
  auto s = __builtin_amdgcn_permlane32_swap(__float_as_uint(a), __float_as_uint(b), false, false);
  return __uint_as_float(s[0]) + __uint_as_float(s[1]);
}
__device__ __forceinline__ float tsum16(float a, float b) {
  auto s = __builtin_amdgcn_permlane16_swap(__float_as_uint(a), __float_as_uint(b), false, false);
  return __uint_as_float(s[0]) + __uint_as_float(s[1]);
}
__device__ __forceinline__ float row_sum(float x) { x += dpp<R8>(x); x += dpp<X7>(x); x += dpp<X2>(x); x += dpp<X1>(x); return x; }
__device__ __forceinline__ float row_max(float x) {
  x = fmaxf(x, dpp<R8>(x)); x = fmaxf(x, dpp<X7>(x)); x = fmaxf(x, dpp<X2>(x)); x = fmaxf(x, dpp<X1>(x)); return x;
}
__device__ __forceinline__ float wave_sum(float x) { x = row_sum(x); x = tsum16(x, x); return tsum32(x, x); }
__device__ __forceinline__ float wave_max(float x) {
  x = row_max(x);
  auto s = __builtin_amdgcn_permlane16_swap(__float_as_uint(x), __float_as_uint(x), false, false);
  x = fmaxf(__uint_as_float(s[0]), __uint_as_float(s[1]));
  auto t = __builtin_amdgcn_permlane32_swap(__float_as_uint(x), __float_as_uint(x), false, false);
  return fmaxf(__uint_as_float(t[0]), __uint_as_float(t[1]));
}
__device__ __forceinline__ rsrc_t mk(const void* p, u32 bytes) { return __builtin_amdgcn_make_buffer_rsrc((void*)p, 0, bytes, 0x00020000); }
__device__ __forceinline__ void bar() {
  asm volatile("s_waitcnt lgkmcnt(0)" ::: "memory");
  __builtin_amdgcn_s_barrier();
  asm volatile("" ::: "memory");
}
__device__ __forceinline__ u64 rt() { return __builtin_amdgcn_s_memrealtime(); }
#define SB() __builtin_amdgcn_sched_barrier(0)

template <typename T> __device__ __forceinline__ LAS T* lp(LAS unsigned char* l, int off) { return (LAS T*)(l + off); }

__device__ __forceinline__ void put_rec(rsrc_t ws, u32 off, u32 d0, u32 d1, u32 T0, u32 T1) {
  __builtin_amdgcn_raw_buffer_store_b128(u32x4{d0, d1, d0 ^ T0, d1 ^ T1}, ws, off, 0, AUX_SC1);
}
template <int N, int SLEEP>
__device__ __forceinline__ void get_recs(rsrc_t ws, const u32 (&off)[N], u32 T0, u32 T1, u32 (&d0)[N], u32 (&d1)[N], volatile LAS u32* dead) {
#pragma unroll 1
  for (u32 spin = 0;; ++spin) {
    asm volatile("" ::: "memory");
    u32x4 t[N];
#pragma unroll
    for (int k = 0; k < N; ++k) t[k] = __builtin_amdgcn_raw_buffer_load_b128(ws, off[k], 0, AUX_SC1);
    SB();
    u32 bad = 0u;
#pragma unroll
    for (int k = 0; k < N; ++k) { bad |= (t[k].x ^ t[k].z ^ T0) | (t[k].y ^ t[k].w ^ T1); d0[k] = t[k].x; d1[k] = t[k].y; }
    if (!__builtin_amdgcn_ballot_w64(bad != 0u)) break;
    if (SLEEP) __builtin_amdgcn_s_sleep(SLEEP);
    if ((spin & 255u) == 255u) { if (*dead) break; if (spin > SPIN_G) { *dead = 1u; break; } }
  }
}
template <int SLEEP>
__device__ __forceinline__ void wait_flag(volatile LAS u32* f, u32 want, volatile LAS u32* dead) {
  for (u32 spin = 0; (u32)__builtin_amdgcn_readfirstlane(*f) < want; ++spin) {
    if (SLEEP) __builtin_amdgcn_s_sleep(SLEEP);
    if ((spin & 255u) == 255u) { if (*dead) break; if (spin > SPIN_L) { *dead = 1u; break; } }
  }
  asm volatile("" ::: "memory");
}
__device__ __forceinline__ void signal_add(LAS u32* f, int lane) {
  asm volatile("s_waitcnt lgkmcnt(0)" ::: "memory");
  if (lane == 0) __hip_atomic_fetch_add(f, 1u, __ATOMIC_RELAXED, __HIP_MEMORY_SCOPE_WORKGROUP);
}

#define STAMP(i) do { } while (0)

template <int SLEEP>
__device__ __forceinline__ void gather_o(rsrc_t ws, LAS unsigned char* L, int t, int xg, int vw, int lane, u32 T0, u32 T1) {
  u32 off[12], d0[12], d1[12];
  const u32 base = WS_X2 + (u32)(((t * 8 + xg) * 32 + 8 * vw + (lane >> 5)) * 96 + (lane & 31)) * 16u;
#pragma unroll
  for (int n = 0; n < 4; ++n)
#pragma unroll
    for (int pr = 0; pr < 3; ++pr) off[n * 3 + pr] = base + (u32)(2 * n * 96 + 32 * pr) * 16u;
  get_recs<12, SLEEP>(ws, off, T0, T1, d0, d1, lp<u32>(L, L_FLAG) + F_DEAD);
#pragma unroll
  for (int pr = 0; pr < 3; ++pr) {
    float a = (__uint_as_float(d0[pr]) + __uint_as_float(d0[3 + pr])) + (__uint_as_float(d0[6 + pr]) + __uint_as_float(d0[9 + pr]));
    float b = (__uint_as_float(d1[pr]) + __uint_as_float(d1[3 + pr])) + (__uint_as_float(d1[6 + pr]) + __uint_as_float(d1[9 + pr]));
    const float s = tsum32(a, b);
    lp<float>(L, L_RED)[((vw * 3 + pr) * 32 + (lane & 31)) * 2 + (lane >> 5)] = s;
  }
}
__device__ __forceinline__ u32 reduce_o(LAS unsigned char* L, int pr, int i) {
  f32x2 r[4];
#pragma unroll
  for (int vw = 0; vw < 4; ++vw) r[vw] = lp<f32x2>(L, L_RED)[(vw * 3 + pr) * 32 + i];
  const f32x2 s = (r[0] + r[1]) + (r[2] + r[3]);
  return pk_bf16(s.x, s.y);
}

__global__ void __launch_bounds__(NTHR, 1) fused_block(Args a) {
  __shared__ __attribute__((aligned(16))) unsigned char lds_raw[LDS_BYTES];
  LAS unsigned char* L = (LAS unsigned char*)lds_raw;
  const int tid = threadIdx.x, lane = tid & 63, w = __builtin_amdgcn_readfirstlane(tid >> 6);
  const int b = blockIdx.x, xg = b & 7, j = b >> 3;
  const int r = lane >> 3, j8 = lane & 7;
  volatile LAS u32* FL = lp<u32>(L, L_FLAG);
  volatile LAS u32* dead = FL + F_DEAD;
  STAMP(0);
  const rsrc_t rk = mk(a.k, HEADB * H), rv = mk(a.v, HEADB * H), ws = mk(a.ws, WS_BYTES);
  const rsrc_t rx = mk(a.x, D * 2), rq = mk(a.wq, D * H * DH * 2);

  const float EPS = 1e-5f;
  u32 eoff = WS_E; asm volatile("" : "+v"(eoff));
  const u32 voff = (u32)r * 384u + (u32)j8 * 16u;
  const u32 sbase = (u32)xg * HEADB + (u32)j * 24576u + (u32)w * 6144u;
  auto step_off = [&](int n) -> u32 { return sbase + (u32)(n >> 4) * (8u * HEADB) + (u32)(n & 15) * (32u * 24576u); };
  u32x2 ev; u32 xme, T0, T1;
  auto make_tag = [&]() {
    u64 z = (((u64)ev.y << 32) | ev.x) + 0x9E3779B97F4A7C15ull;
    z = (z ^ (z >> 30)) * 0xBF58476D1CE4E5B9ull; z = (z ^ (z >> 27)) * 0x94D049BB133111EBull; z ^= z >> 31;
    T0 = __builtin_amdgcn_readfirstlane((u32)z) | 1u; T1 = __builtin_amdgcn_readfirstlane((u32)(z >> 32)) | 1u;
  };

  u32x4 gw[4]; u32x2 dw[8];
  auto load_ffn = [&]() {
    const rsrc_t rg = mk(a.wg, D * F * 2), ru = mk(a.wu, D * F * 2), rd = mk(a.wd, F * D * 2);
    const u32 og = j < 24 ? (u32)tid * (2u * F * 2u) + 384u * xg + 16u * j : OOB;
    gw[0] = __builtin_amdgcn_raw_buffer_load_b128(rg, og, 0, 0);
    gw[1] = __builtin_amdgcn_raw_buffer_load_b128(rg, og, F * 2, 0);
    gw[2] = __builtin_amdgcn_raw_buffer_load_b128(ru, og, 0, 0);
    gw[3] = __builtin_amdgcn_raw_buffer_load_b128(ru, og, F * 2, 0);
    const u32 od = (j >= 12 && tid < 192) ? (u32)tid * (8u * D * 2u) + 160u * xg + 8u * (j - 12) : OOB;
#pragma unroll
    for (int n = 0; n < 8; ++n) dw[n] = __builtin_amdgcn_raw_buffer_load_b64(rd, od, n * D * 2, 0);
  };
  u32 gp[16], dp[16];
  auto pack_ffn = [&]() {
#pragma unroll
    for (int e = 0; e < 4; ++e) {
      gp[2 * e] = zip_lo(gw[0][e], gw[1][e]); gp[2 * e + 1] = zip_hi(gw[0][e], gw[1][e]);
      gp[8 + 2 * e] = zip_lo(gw[2][e], gw[3][e]); gp[8 + 2 * e + 1] = zip_hi(gw[2][e], gw[3][e]);
    }
#pragma unroll
    for (int n = 0; n < 4; ++n)
#pragma unroll
      for (int e = 0; e < 2; ++e) { dp[4 * n + 2 * e] = zip_lo(dw[2 * n][e], dw[2 * n + 1][e]); dp[4 * n + 2 * e + 1] = zip_hi(dw[2 * n][e], dw[2 * n + 1][e]); }
#pragma unroll
    for (int e = 0; e < 16; ++e) asm volatile("" : "+v"(gp[e]), "+v"(dp[e]));
  };

  if (w == SVC) {
    ev = __builtin_amdgcn_raw_buffer_load_b64(ws, eoff, 0, AUX_SC1);
    xme = __builtin_amdgcn_raw_buffer_load_b32(rx, tid * 4, 0, 0);
    if (lane < 16) FL[lane] = 0u;
    make_tag();
    STAMP(1);
    bar();
    {
      f32x4 s = {0.f, 0.f, 0.f, 0.f};
      if (lane < 6) {
#pragma unroll
        for (int ww = 0; ww < 4; ++ww) s += lp<f32x4>(L, L_QP)[ww * 6 + lane];
      }
      put_rec(ws, lane < 6 ? WS_X0 + (u32)(xg * 192 + j * 6 + lane) * 16u : OOB, pk_bf16(s.x, s.y), pk_bf16(s.z, s.w), T0, T1);
      __builtin_amdgcn_s_sleep(16);
      u32 off[3], d0[3], d1[3];
#pragma unroll
      for (int k = 0; k < 3; ++k) off[k] = WS_X0 + (u32)(xg * 192 + lane + 64 * k) * 16u;
      __builtin_amdgcn_s_barrier();
      get_recs<3, 0>(ws, off, T0, T1, d0, d1, dead);
#pragma unroll
      for (int k = 0; k < 3; ++k) lp<u32x2>(L, L_Q)[lane + 64 * k] = u32x2{d0[k], d1[k]};
    }
    STAMP(2);
    bar();
    load_ffn();
    pack_ffn();
#pragma unroll 1
    for (int t = 0; t < 4; ++t) {
      wait_flag<8>(FL + F_K + t, 4u, dead);
      LAS u32x4* sp = lp<u32x4>(L, L_S + ((lane >> 4) * 4 + t) * 512 + (lane & 15) * 32);
      const u32x4 s0 = sp[0], s1 = sp[1];
      float s[16];
#pragma unroll
      for (int e = 0; e < 4; ++e) { s[2 * e] = lo_f(s0[e]); s[2 * e + 1] = hi_f(s0[e]); s[8 + 2 * e] = lo_f(s1[e]); s[8 + 2 * e + 1] = hi_f(s1[e]); }
      float m = s[0];
#pragma unroll
      for (int e = 1; e < 16; ++e) m = fmaxf(m, s[e]);
      m = wave_max(m);
      const float L2E = 1.4426950408889634f;
      float l = 0.f;
#pragma unroll
      for (int e = 0; e < 16; ++e) l += __builtin_amdgcn_exp2f((s[e] - m) * L2E);
      l = wave_sum(l);
      put_rec(ws, lane == 0 ? WS_X1 + (u32)((t * 8 + xg) * 32 + j) * 16u : OOB, __float_as_uint(m), __float_as_uint(l), T0, T1);
      u32 off[1] = {WS_X1 + (u32)((t * 8 + xg) * 32 + (lane & 31)) * 16u}, d0[1], d1[1];
      __builtin_amdgcn_s_sleep(32);
      get_recs<1, 32>(ws, off, T0, T1, d0, d1, dead);
      const float mj = __uint_as_float(d0[0]), lj = __uint_as_float(d1[0]);
      const float M = wave_max(mj);
      float Ls = row_sum(lj * __builtin_amdgcn_exp2f((mj - M) * L2E));
      Ls = tsum16(Ls, Ls);
      Ls = __uint_as_float(__builtin_amdgcn_readfirstlane(__float_as_uint(Ls)));
      const float inv = 1.f / Ls;
      u32x4 p0, p1;
#pragma unroll
      for (int e = 0; e < 4; ++e) {
        p0[e] = pk_bf16(__builtin_amdgcn_exp2f((s[2 * e] - M) * L2E) * inv, __builtin_amdgcn_exp2f((s[2 * e + 1] - M) * L2E) * inv);
        p1[e] = pk_bf16(__builtin_amdgcn_exp2f((s[8 + 2 * e] - M) * L2E) * inv, __builtin_amdgcn_exp2f((s[8 + 2 * e + 1] - M) * L2E) * inv);
      }
      sp[0] = p0; sp[1] = p1;
      signal_add((LAS u32*)FL + F_P + t, lane);
    }
#pragma unroll 1
    for (int t = 0; t < 4; ++t) {
      if (t < 3) wait_flag<8>(FL + F_V + t, 4u, dead); else wait_flag<0>(FL + F_V + t, 4u, dead);
      f32x4 s = {0.f, 0.f, 0.f, 0.f};
      if (lane < 48) {
        const LAS f32x4* pp = lp<f32x4>(L, L_PART + t * 3072) + lane;
        s = (pp[0] + pp[48]) + (pp[96] + pp[144]);
      }
      const u32 o = lane < 48 ? WS_X2 + (u32)(((t * 8 + xg) * 32 + j) * 96 + 2 * lane) * 16u : OOB;
      put_rec(ws, o, __float_as_uint(s.x), __float_as_uint(s.y), T0, T1);
      put_rec(ws, o + 16u, __float_as_uint(s.z), __float_as_uint(s.w), T0, T1);
      if (t < 3) {
        __builtin_amdgcn_s_sleep(100);
#pragma unroll 1
        for (int vw = 0; vw < 4; ++vw) gather_o<64>(ws, L, t, xg, vw, lane, T0, T1);
        asm volatile("s_waitcnt lgkmcnt(0)" ::: "memory");
        if (lane < 32) {
#pragma unroll
          for (int pr = 0; pr < 3; ++pr) lp<u32>(L, L_OBF)[t * 96 + 32 * pr + lane] = reduce_o(L, pr, lane);
        }
        if (t == 2) signal_add((LAS u32*)FL + F_O, lane);
      }
    }
    STAMP(6);
  } else {
    u32 xs[5]; u16 xr[10]; u32x3 wqv[10]; u32x4 ring[2][3]; u32x2 wov[16];
#pragma unroll
    for (int m = 0; m < 5; ++m) xs[m] = __builtin_amdgcn_raw_buffer_load_b32(rx, (lane + 64 * m) * 4, 0, 0);
    {
      const u32 col = (u32)((8 * (j >> 3) + xg) * DH + 24 * (j & 7)) * 2u + 12u * (lane & 3);
      const u32 o = (u32)(160 * w + (lane >> 2)) * (u32)(H * DH * 2) + col;
#pragma unroll
      for (int i = 0; i < 10; ++i) wqv[i] = __builtin_amdgcn_raw_buffer_load_b96(rq, o, i * 16 * (H * DH * 2), 0);
    }
#pragma unroll
    for (int i = 0; i < 10; ++i) xr[i] = __builtin_amdgcn_raw_buffer_load_b16(rx, (160 * w + 16 * i + (lane >> 2)) * 2, 0, 0);
    ev = __builtin_amdgcn_raw_buffer_load_b64(ws, eoff, 0, AUX_SC1);
    xme = __builtin_amdgcn_raw_buffer_load_b32(rx, tid * 4, 0, 0);
    SB();
#pragma unroll
    for (int g = 0; g < 2; ++g)
#pragma unroll
      for (int i = 0; i < 3; ++i) ring[g][i] = __builtin_amdgcn_raw_buffer_load_b128(rk, voff + g * 3072 + i * 128, sbase, AUX_NT);
    SB();
    {
      const rsrc_t ro = mk(a.wo, H * DH * D * 2);
      const int l5 = (lane * 52) >> 8;
      const u32 oo = (u32)((8 * w + xg) * DH + l5) * (D * 2) + 40u * j + 8u * (lane - 5 * l5);
      const u32 o = lane < 60 ? oo : OOB;
#pragma unroll
      for (int n = 0; n < 16; ++n) wov[n] = __builtin_amdgcn_raw_buffer_load_b64(ro, o, n * 12 * D * 2, 0);
    }
    SB();
    {
      float s = 0.f;
#pragma unroll
      for (int m = 0; m < 5; ++m) s += lo_f(xs[m]) + hi_f(xs[m]);
      const float mean = wave_sum(s) * (1.f / D);
      float q2 = 0.f;
#pragma unroll
      for (int m = 0; m < 5; ++m) { const float d0 = lo_f(xs[m]) - mean, d1 = hi_f(xs[m]) - mean; q2 += d0 * d0 + d1 * d1; }
      const float var = rbf(wave_sum(q2) * (1.f / D));
      const float mb = rbf(mean), inv = 1.f / rbf(__builtin_sqrtf(rbf(var + EPS)));
      float acc[6] = {0.f, 0.f, 0.f, 0.f, 0.f, 0.f};
#pragma unroll
      for (int i = 0; i < 10; ++i) {
        const float xh = rbf(rbf(lo_f(xr[i]) - mb) * inv);
#pragma unroll
        for (int e = 0; e < 3; ++e) { acc[2 * e] += xh * lo_f(wqv[i][e]); acc[2 * e + 1] += xh * hi_f(wqv[i][e]); }
      }
#pragma unroll
      for (int e = 0; e < 3; ++e) {
        float t = tsum32(acc[e], acc[e + 3]);
        t = tsum16(t, t); t += dpp<R8>(t); t += dpp<R4>(t);
        if ((lane & 28) == 0) lp<float>(L, L_QP)[w * 24 + 6 * (lane & 3) + 3 * (lane >> 5) + e] = t;
      }
    }
    STAMP(1);
    bar();
    make_tag();
    __builtin_amdgcn_s_barrier();
    load_ffn();
    SB();
    bar();
    STAMP(2);
#define PACE_STEP() do { } while (0)
    u32x4 q[3];
    LAS u32* sp = lp<u32>(L, L_S + w * 2048) + r;
    const float SCALE = 0.07216878364870322f;
    auto k_step = [&](int n, bool last, u32 so) {
      float d[2];
#pragma unroll
      for (int g = 0; g < 2; ++g) {
        float d0 = 0.f, d1 = 0.f;
#pragma unroll
        for (int i = 0; i < 3; ++i) {
          d0 = dot2(ring[g][i].x, q[i].x, d0); d1 = dot2(ring[g][i].y, q[i].y, d1);
          d0 = dot2(ring[g][i].z, q[i].z, d0); d1 = dot2(ring[g][i].w, q[i].w, d1);
        }
        d[g] = d0 + d1;
        asm volatile("" : "+v"(d[g]));
        SB();
        if (!last) {
#pragma unroll
          for (int i = 0; i < 3; ++i) ring[g][i] = __builtin_amdgcn_raw_buffer_load_b128(rk, voff + g * 3072 + i * 128, so, AUX_NT);
        }
        SB();
      }
      if (last) {
#pragma unroll
        for (int i = 0; i < 3; ++i) {
          ring[0][i] = __builtin_amdgcn_raw_buffer_load_b128(rv, voff + i * 128, so, AUX_NT);
          SB();
          ring[1][i] = __builtin_amdgcn_raw_buffer_load_b128(rv, voff + 3072 + i * 128, so, AUX_NT);
          SB();
        }
      }
#pragma unroll
      for (int g = 0; g < 2; ++g) { d[g] += dpp<X1>(d[g]); d[g] += dpp<X2>(d[g]); d[g] += dpp<X7>(d[g]); }
      const u32 s1 = pk_bf16(d[0], d[1]);
      const u32 s2 = pk_bf16(lo_f(s1) * SCALE, hi_f(s1) * SCALE);
      if (j8 == 0) sp[n * 8] = s2;
    };
    auto load_q = [&](int t) {
#pragma unroll
      for (int i = 0; i < 3; ++i) q[i] = lp<u32x4>(L, L_Q + t * 384)[j8 + 8 * i];
    };
    load_q(0);
    k_step(0, false, step_off(1));
    if (lane < 60) {
#pragma unroll
      for (int n = 0; n < 16; ++n) lp<u32x2>(L, L_WO + (w * 192 + 12 * n) * 40)[lane] = wov[n];
    }
    pack_ffn();
    SB();
    for (int n = 1; n < 63; ++n) {
      if ((n & 15) == 0) { signal_add((LAS u32*)FL + F_K + (n >> 4) - 1, lane); load_q(n >> 4); }
      PACE_STEP();
      k_step(n, false, step_off(n + 1));
    }
    PACE_STEP();
    k_step(63, true, step_off(0));
    signal_add((LAS u32*)FL + F_K + 3, lane);
    STAMP(3);

    float acc[24];
    auto v_step = [&](u32 pp, u32 vo, u32 so) {
#pragma unroll
      for (int i = 0; i < 3; ++i) {
#pragma unroll
        for (int e = 0; e < 4; ++e) {
          acc[i * 8 + 2 * e] = dot2(zip_lo(ring[0][i][e], ring[1][i][e]), pp, acc[i * 8 + 2 * e]);
          acc[i * 8 + 2 * e + 1] = dot2(zip_hi(ring[0][i][e], ring[1][i][e]), pp, acc[i * 8 + 2 * e + 1]);
        }
#pragma unroll
        for (int e = 0; e < 8; ++e) asm volatile("" : "+v"(acc[i * 8 + e]));
        SB();
        ring[0][i] = __builtin_amdgcn_raw_buffer_load_b128(rv, vo + i * 128, so, AUX_NT);
        ring[1][i] = __builtin_amdgcn_raw_buffer_load_b128(rv, vo + 3072 + i * 128, so, AUX_NT);
        SB();
      }
    };
#pragma unroll 1
    for (int t = 0; t < 4; ++t) {
      wait_flag<1>(FL + F_P + t, 1u, dead);
#pragma unroll
      for (int e = 0; e < 24; ++e) acc[e] = 0.f;
      u32 pp = sp[t * 128];
#pragma unroll 1
      for (int st = 0; st < 16; ++st) {
        const int n = t * 16 + st;
        const u32 pn = sp[(n + (st < 15 ? 1 : 0)) * 8];
        PACE_STEP();
        v_step(pp, n < 63 ? voff : OOB, step_off(n < 63 ? n + 1 : 0));
        pp = pn;
      }
      if (t == 3) STAMP(4);
#pragma unroll
      for (int i = 0; i < 3; ++i) {
        float s1[4], s2[2];
#pragma unroll
        for (int e = 0; e < 4; ++e) s1[e] = tsum32(acc[i * 8 + e], acc[i * 8 + e + 4]);
#pragma unroll
        for (int e = 0; e < 2; ++e) s2[e] = tsum16(s1[e], s1[e + 2]);
        const bool b3 = (lane & 8) != 0;
        const float keep = b3 ? s2[1] : s2[0], send = b3 ? s2[0] : s2[1];
        lp<float>(L, L_PART + (t * 4 + w) * 768)[64 * i + 8 * j8 + r] = keep + dpp<R8>(send);
      }
      signal_add((LAS u32*)FL + F_V + t, lane);
    }
    STAMP(5);
  }

  const int hw = tid >> 5, i32 = tid & 31;
  float y0 = 0.f, y1 = 0.f;
  u32 w3[6];
  wait_flag<1>(FL + F_O, 1u, dead);
#pragma unroll
  for (int t = 0; t < 4; ++t)
#pragma unroll
    for (int pr = 0; pr < 3; ++pr) {
      const LAS u32* wp = lp<u32>(L, L_WO) + (t * 192 + 2 * (i32 + 32 * pr)) * 10 + hw;
      const u32 A = wp[0], B = wp[10];
      const u32 w0 = zip_lo(A, B), w1 = zip_hi(A, B);
      if (t < 3) { const u32 op = lp<u32>(L, L_OBF)[t * 96 + 32 * pr + i32]; y0 = dot2(w0, op, y0); y1 = dot2(w1, op, y1); }
      else { w3[2 * pr] = w0; w3[2 * pr + 1] = w1; }
    }
  asm volatile("" : "+v"(y0), "+v"(y1));
  if (w != SVC) gather_o<0>(ws, L, 3, xg, w, lane, T0, T1);
  STAMP(7);
  bar();
#pragma unroll
  for (int pr = 0; pr < 3; ++pr) { const u32 op = reduce_o(L, pr, i32); y0 = dot2(w3[2 * pr], op, y0); y1 = dot2(w3[2 * pr + 1], op, y1); }
  {
    const float s = row_sum(tsum16(y0, y1));
    auto p = __builtin_amdgcn_permlane16_swap(__float_as_uint(s), __float_as_uint(s), false, false);
    put_rec(ws, i32 == 0 ? WS_X3 + (u32)(xg * 320 + 10 * j + hw) * 16u : OOB, p[0], p[1], T0, T1);
  }
  STAMP(8);
  u32 hp;
  {
    u32 off[8], d0[8], d1[8];
#pragma unroll
    for (int k = 0; k < 8; ++k) off[k] = WS_X3 + (u32)(k * 320 + tid) * 16u;
    get_recs<8, 0>(ws, off, T0, T1, d0, d1, dead);
    float s0 = 0.f, s1 = 0.f;
#pragma unroll
    for (int k = 0; k < 8; ++k) { s0 += __uint_as_float(d0[k]); s1 += __uint_as_float(d1[k]); }
    const u32 at = pk_bf16(s0, s1);
    hp = pk_bf16(lo_f(xme) + lo_f(at), hi_f(xme) + hi_f(at));
  }
  STAMP(9);
  const float h0 = lo_f(hp), h1 = hi_f(hp);
  {
    const float mw = wave_sum(h0 + h1) * (1.f / 128.f);
    const float m2 = wave_sum((h0 - mw) * (h0 - mw) + (h1 - mw) * (h1 - mw));
    lp<u32>(L, L_H)[tid] = hp;
    if (lane == 0) lp<f32x2>(L, L_ST)[w] = f32x2{mw, m2};
  }
  bar();
  if (b == 0 && tid == 0) {
    const u64 e1 = (((u64)ev.y << 32) | ev.x) + 1ull;
    __builtin_amdgcn_raw_buffer_store_b64(u32x2{(u32)e1, (u32)(e1 >> 32)}, ws, WS_E, 0, AUX_SC1);
  }
  if (j < 24) {
    f32x2 st[5];
#pragma unroll
    for (int ww = 0; ww < 5; ++ww) st[ww] = lp<f32x2>(L, L_ST)[ww];
    const float mean = (((st[0].x + st[1].x) + (st[2].x + st[3].x)) + st[4].x) * 0.2f;
    float m2 = 0.f;
#pragma unroll
    for (int ww = 0; ww < 5; ++ww) m2 += st[ww].y + 128.f * (st[ww].x - mean) * (st[ww].x - mean);
    const float var = rbf(m2 * (1.f / D));
    const float mb = rbf(mean), inv = 1.f / rbf(__builtin_sqrtf(rbf(var + EPS)));
    const u32 dd = pk_bf16(h0 - mb, h1 - mb);
    const u32 hn = pk_bf16(lo_f(dd) * inv, hi_f(dd) * inv);
    float g[16];
#pragma unroll
    for (int e = 0; e < 16; ++e) g[e] = dot2(gp[e], hn, 0.f);
    float g8[8], g4[4], g2[2];
#pragma unroll
    for (int e = 0; e < 8; ++e) g8[e] = tsum32(g[e], g[e + 8]);
#pragma unroll
    for (int e = 0; e < 4; ++e) g4[e] = tsum16(g8[e], g8[e + 4]);
    const bool b3 = (lane & 8) != 0, b2 = (lane & 4) != 0;
#pragma unroll
    for (int e = 0; e < 2; ++e) g2[e] = (b3 ? g4[e + 2] : g4[e]) + dpp<R8>(b3 ? g4[e] : g4[e + 2]);
    float g1 = (b2 ? g2[1] : g2[0]) + dpp<X7>(b2 ? g2[0] : g2[1]);
    g1 += dpp<X2>(g1); g1 += dpp<X1>(g1);
    if ((lane & 3) == 0) lp<float>(L, L_GU)[w * 16 + (lane >> 2)] = g1;
    bar();
    if (w == 0) {
      const int c = lane & 7;
      float gs = 0.f, us = 0.f;
#pragma unroll
      for (int ww = 0; ww < 5; ++ww) { gs += lp<float>(L, L_GU)[ww * 16 + c]; us += lp<float>(L, L_GU)[ww * 16 + 8 + c]; }
      const u32 gu = pk_bf16(gs, us);
      const float sg = rbf(1.f / (1.f + __builtin_amdgcn_exp2f(-1.4426950408889634f * lo_f(gu))));
      const float f = sg * hi_f(gu);
      const u32 d0 = pk_bf16(dpp<0x00>(f), dpp<0x55>(f)), d1 = pk_bf16(dpp<0xAA>(f), dpp<0xFF>(f));
      put_rec(ws, (lane & 59) == 0 ? WS_X4 + (u32)((xg * 24 + j) * 2 + (lane >> 2)) * 16u : OOB, d0, d1, T0, T1);
    }
  }
  STAMP(10);
  if (j >= 12) {
    if (w < 3) {
      u32 off[2] = {WS_X4 + (u32)tid * 32u, WS_X4 + (u32)tid * 32u + 16u}, d0[2], d1[2];
      get_recs<2, 0>(ws, off, T0, T1, d0, d1, dead);
      STAMP(11);
      float c[4];
#pragma unroll
      for (int e = 0; e < 4; ++e) c[e] = dot2(dp[12 + e], d1[1], dot2(dp[8 + e], d0[1], dot2(dp[4 + e], d1[0], dot2(dp[e], d0[0], 0.f))));
      const float s = row_sum(tsum16(tsum32(c[0], c[2]), tsum32(c[1], c[3])));
      if ((lane & 15) == 0) lp<float>(L, L_DN)[w * 4 + (lane >> 4)] = s;
    }
    bar();
    if (tid < 4) {
      const float s = rbf((lp<float>(L, L_DN)[tid] + lp<float>(L, L_DN)[4 + tid]) + lp<float>(L, L_DN)[8 + tid]);
      const int col = 80 * xg + 4 * (j - 12) + tid;
      const float hv = lo_f(lp<u16>(L, L_H)[col]);
      a.out[col] = (u16)pk_bf16(hv + s, 0.f);
    }
  }
  STAMP(12);
}
}

extern "C" void kernel_launch(void* const* d_in, const int* in_sizes, int n_in, void* d_out, int out_size, void* d_ws, size_t ws_size,
                              hipStream_t stream) {
  Args a;
  a.x = (const u16*)d_in[0]; a.k = (const u16*)d_in[1]; a.v = (const u16*)d_in[2]; a.wq = (const u16*)d_in[3]; a.wo = (const u16*)d_in[4];
  a.wg = (const u16*)d_in[6]; a.wu = (const u16*)d_in[7]; a.wd = (const u16*)d_in[8];
  a.out = (u16*)d_out; a.ws = (unsigned char*)d_ws;
  hipLaunchKernelGGL(fused_block, dim3(256), dim3(NTHR), 0, stream, a);
}
```

```cpp
#include <hip/hip_runtime.h>
#include <hip/hip_bf16.h>
#include <stdint.h>

#define STAMPS 0
#define PACE 1340
#define RING 6
#define PFT 1
#define BG 2
#define SENT_A 1
#define SENT_B 1
#define SLP_A 0
#define SLP_B 0
#define SLP_C 2
#define NOPF 0
#define DUTY0 40
#define DUTY1 10
#define DUTY2 32
#define PFWAIT 1
#define CPF 3
#define CPB 14
#define QDELAY 32
#define PRE 10
#define PACE_BURST 5000

typedef unsigned u32;
typedef unsigned long long u64;
typedef unsigned short u16;
typedef u32 u32x2 __attribute__((ext_vector_type(2)));
typedef u32 u32x3 __attribute__((ext_vector_type(3)));
typedef u32 u32x4 __attribute__((ext_vector_type(4)));
typedef float f32x2 __attribute__((ext_vector_type(2)));
typedef float f32x4 __attribute__((ext_vector_type(4)));
typedef __bf16 bf16x2 __attribute__((ext_vector_type(2)));
typedef __amdgpu_buffer_rsrc_t rsrc_t;
#define LAS __attribute__((address_space(3)))

namespace {
constexpr int D = 640, H = 32, SK = 32768, DH = 192, F = 1536;
constexpr u32 HEADB = (u32)SK * DH * 2;
constexpr int NTHR = 320, SVC = 4;
constexpr u32 OOB = 0x80000000u;
constexpr int AUX_NT = 2, AUX_SC1 = 16;
constexpr u32 WS_E = 0, WS_X1 = 4096, WS_X2 = WS_X1 + 4 * 8 * 32 * 128, WS_X0 = WS_X2 + 4 * 8 * 32 * 1536, WS_XC = WS_X0 + 8 * 32 * 1536,
              WS_XO = WS_XC + 8 * 24 * 640, WS_X3 = WS_XO + 8 * 24 * 128, X3_REP = 8 * 320 * 16, WS_X4 = WS_X3 + 8 * X3_REP, X4_REP = 192 * 128, WS_BYTES = WS_X4 + 8 * X4_REP;
constexpr u32 WS_STAMP = 4u << 20;
static_assert(WS_BYTES <= WS_STAMP && WS_XC % 128 == 0 && WS_X3 % 128 == 0 && WS_X4 % 128 == 0, "ws");
constexpr int L_Q = 0, L_Y = 1536, L_FLAG = 2048, L_ST = 2176, L_GU = 2240, L_DN = 2560, L_H = 2752, L_OB = 3072, L_RED = 5120,
              L_S = 8192, L_PART = 16384, L_WO = 28672, L_WP = 59392, LDS_BYTES = L_WP + 30720;
constexpr int F_K = 0, F_P = 4, F_V = 8, F_O = 12, F_DEAD = 13, F_Q = 14, F_L = 15, F_BLK = 16;
constexpr u32 SPIN_G = 1u << 17, SPIN_L = 1u << 21;

struct Args {
  const u16 *x, *k, *v, *wq, *wo, *wg, *wu, *wd;
  u16* out;
  unsigned char* ws;
};

__device__ __forceinline__ float dot2(u32 a, u32 b, float c) {
  return __builtin_amdgcn_fdot2_f32_bf16(__builtin_bit_cast(bf16x2, a), __builtin_bit_cast(bf16x2, b), c, false);
}
__device__ __forceinline__ u32 pk_bf16(float lo, float hi) {
  bf16x2 r = {(__bf16)lo, (__bf16)hi};
  return __builtin_bit_cast(u32, r);
}
__device__ __forceinline__ float lo_f(u32 a) { return __uint_as_float(a << 16); }
__device__ __forceinline__ float hi_f(u32 a) { return __uint_as_float(a & 0xffff0000u); }
__device__ __forceinline__ float rbf(float a) { return lo_f(pk_bf16(a, a)); }
__device__ __forceinline__ u32 zip_lo(u32 a, u32 b) { return __builtin_amdgcn_perm(b, a, 0x05040100u); }
__device__ __forceinline__ u32 zip_hi(u32 a, u32 b) { return __builtin_amdgcn_perm(b, a, 0x07060302u); }
template <int CTRL> __device__ __forceinline__ float dpp(float x) {
  return __builtin_bit_cast(float, __builtin_amdgcn_mov_dpp(__builtin_bit_cast(int, x), CTRL, 0xf, 0xf, true));
}
constexpr int X1 = 0xB1, X2 = 0x4E, X7 = 0x141, R1 = 0x121, R2 = 0x122, R4 = 0x124, R8 = 0x128;
__device__ __forceinline__ float tsum32(float a, float b) {
  auto s = __builtin_amdgcn_permlane32_swap(__float_as_uint(a), __float_as_uint(b), false, false);
  return __uint_as_float(s[0]) + __uint_as_float(s[1]);
}
__device__ __forceinline__ float tsum16(float a, float b) {
  auto s = __builtin_amdgcn_permlane16_swap(__float_as_uint(a), __float_as_uint(b), false, false);
  return __uint_as_float(s[0]) + __uint_as_float(s[1]);
}
__device__ __forceinline__ float row_sum(float x) { x += dpp<R8>(x); x += dpp<X7>(x); x += dpp<X2>(x); x += dpp<X1>(x); return x; }
__device__ __forceinline__ float row_max(float x) {
  x = fmaxf(x, dpp<R8>(x)); x = fmaxf(x, dpp<X7>(x)); x = fmaxf(x, dpp<X2>(x)); x = fmaxf(x, dpp<X1>(x)); return x;
}
__device__ __forceinline__ float wave_sum(float x) { x = row_sum(x); x = tsum16(x, x); return tsum32(x, x); }
__device__ __forceinline__ float wave_max(float x) {
  x = row_max(x);
  auto s = __builtin_amdgcn_permlane16_swap(__float_as_uint(x), __float_as_uint(x), false, false);
  x = fmaxf(__uint_as_float(s[0]), __uint_as_float(s[1]));
  auto t = __builtin_amdgcn_permlane32_swap(__float_as_uint(x), __float_as_uint(x), false, false);
  return fmaxf(__uint_as_float(t[0]), __uint_as_float(t[1]));
}
__device__ __forceinline__ rsrc_t mk(const void* p, u32 bytes) { return __builtin_amdgcn_make_buffer_rsrc((void*)p, 0, bytes, 0x00020000); }
__device__ __forceinline__ void bar() {
  asm volatile("s_waitcnt lgkmcnt(0)" ::: "memory");
  __builtin_amdgcn_s_barrier();
  asm volatile("" ::: "memory");
}
__device__ __forceinline__ u64 rt() { return __builtin_amdgcn_s_memrealtime(); }
#define SB() __builtin_amdgcn_sched_barrier(0)

template <typename T> __device__ __forceinline__ LAS T* lp(LAS unsigned char* l, int off) { return (LAS T*)(l + off); }

__device__ __forceinline__ void put_rec(rsrc_t ws, u32 off, u32 d0, u32 d1, u32 T0, u32 T1) {
  __builtin_amdgcn_raw_buffer_store_b128(u32x4{d0, d1, d0 ^ T0, d1 ^ T1}, ws, off, 0, AUX_SC1);
}
template <int N, int SLEEP>
__device__ __forceinline__ void get_recs(rsrc_t ws, const u32 (&off)[N], u32 T0, u32 T1, u32 (&d0)[N], u32 (&d1)[N], volatile LAS u32* dead) {
#pragma unroll 1
  for (u32 spin = 0;; ++spin) {
    asm volatile("" ::: "memory");
    u32x4 t[N];
#pragma unroll
    for (int k = 0; k < N; ++k) t[k] = __builtin_amdgcn_raw_buffer_load_b128(ws, off[k], 0, AUX_SC1);
    SB();
    u32 bad = 0u;
#pragma unroll
    for (int k = 0; k < N; ++k) { bad |= (t[k].x ^ t[k].z ^ T0) | (t[k].y ^ t[k].w ^ T1); d0[k] = t[k].x; d1[k] = t[k].y; }
    if (!__builtin_amdgcn_ballot_w64(bad != 0u)) break;
    if (SLEEP) __builtin_amdgcn_s_sleep(SLEEP);
    if ((spin & 255u) == 255u) { if (*dead) break; if (spin > SPIN_G) { *dead = 1u; break; } }
  }
}
template <int SLEEP>
__device__ __forceinline__ void wait_flag(volatile LAS u32* f, u32 want, volatile LAS u32* dead) {
  for (u32 spin = 0; (u32)__builtin_amdgcn_readfirstlane(*f) < want; ++spin) {
    if (SLEEP) __builtin_amdgcn_s_sleep(SLEEP);
    if ((spin & 255u) == 255u) { if (*dead) break; if (spin > SPIN_L) { *dead = 1u; break; } }
  }
  asm volatile("" ::: "memory");
}
__device__ __forceinline__ void signal_add(LAS u32* f, int lane) {
  asm volatile("s_waitcnt lgkmcnt(0)" ::: "memory");
  if (lane == 0) __hip_atomic_fetch_add(f, 1u, __ATOMIC_RELAXED, __HIP_MEMORY_SCOPE_WORKGROUP);
}

#define STAMP(i) do { } while (0)

template <int G, int SLEEP>
__device__ __forceinline__ void gather8(rsrc_t ws, LAS unsigned char* L, u32 first, int lane, u32 T0, u32 T1, float (&s)[G][3]) {
  u32 off[12 * G], d0[12 * G], d1[12 * G];
  const u32 base = first + (u32)((lane >> 5) * 96 + (lane & 31)) * 16u;
#pragma unroll
  for (int n = 0; n < 4 * G; ++n)
#pragma unroll
    for (int pr = 0; pr < 3; ++pr) off[n * 3 + pr] = base + (u32)(2 * n * 96 + 32 * pr) * 16u;
  get_recs<12 * G, SLEEP>(ws, off, T0, T1, d0, d1, lp<u32>(L, L_FLAG) + F_DEAD);
#pragma unroll
  for (int g = 0; g < G; ++g)
#pragma unroll
    for (int pr = 0; pr < 3; ++pr) {
      const int k = 12 * g + pr;
      float a = (__uint_as_float(d0[k]) + __uint_as_float(d0[k + 3])) + (__uint_as_float(d0[k + 6]) + __uint_as_float(d0[k + 9]));
      float b = (__uint_as_float(d1[k]) + __uint_as_float(d1[k + 3])) + (__uint_as_float(d1[k + 6]) + __uint_as_float(d1[k + 9]));
      s[g][pr] = tsum32(a, b);
    }
}
template <int G, int SLEEP>
__device__ __forceinline__ void gather_o(rsrc_t ws, LAS unsigned char* L, int t, int xg, int vw, int lane, u32 T0, u32 T1) {
  float s[G][3];
  gather8<G, SLEEP>(ws, L, WS_X2 + (u32)(((t * 8 + xg) * 32 + 8 * vw) * 96) * 16u, lane, T0, T1, s);
#pragma unroll
  for (int g = 0; g < G; ++g)
#pragma unroll
    for (int pr = 0; pr < 3; ++pr) lp<float>(L, L_RED)[(((vw + g) * 3 + pr) * 32 + (lane & 31)) * 2 + (lane >> 5)] = s[g][pr];
}
__device__ __forceinline__ void flush24(const float (&acc)[24], LAS float* dst, int lane) {
#pragma unroll
  for (int i = 0; i < 3; ++i) {
    float s1[4], s2[2];
#pragma unroll
    for (int e = 0; e < 4; ++e) s1[e] = tsum32(acc[i * 8 + e], acc[i * 8 + e + 4]);
#pragma unroll
    for (int e = 0; e < 2; ++e) s2[e] = tsum16(s1[e], s1[e + 2]);
    const bool b3 = (lane & 8) != 0;
    const float keep = b3 ? s2[1] : s2[0], send = b3 ? s2[0] : s2[1];
    dst[64 * i + 8 * (lane & 7) + (lane >> 3)] = keep + dpp<R8>(send);
  }
}
__device__ __forceinline__ void publish192(rsrc_t ws, const LAS f32x2* src, u32 first, int lane, u32 T0, u32 T1) {
  const LAS f32x2* p0 = src + lane; const LAS f32x2* p1 = src + 64 + (lane & 31);
  const f32x2 s0 = (p0[0] + p0[96]) + (p0[192] + p0[288]), s1 = (p1[0] + p1[96]) + (p1[192] + p1[288]);
  put_rec(ws, first + (u32)lane * 16u, __float_as_uint(s0.x), __float_as_uint(s0.y), T0, T1);
  put_rec(ws, lane < 32 ? first + 1024u + (u32)lane * 16u : OOB, __float_as_uint(s1.x), __float_as_uint(s1.y), T0, T1);
}
__device__ __forceinline__ void pack_wo(LAS unsigned char* L, int vw, int lane) {
  const LAS u32* src = lp<u32>(L, L_WO) + (lane >> 3) * 960 + 8 * vw + (lane & 7);
#pragma unroll
  for (int m = 0; m < 12; m += 2) {
    const u32 A = src[80 * m], B = src[80 * m + 40], C = src[80 * m + 80], E = src[80 * m + 120];
    lp<u32x4>(L, L_WP)[(vw * 6 + m / 2) * 64 + lane] = u32x4{zip_lo(A, B), zip_hi(A, B), zip_lo(C, E), zip_hi(C, E)};
  }
}
__device__ __forceinline__ void load_wo(LAS unsigned char* L, int vw, int lane, u32x4 (&wp)[6]) {
#pragma unroll
  for (int m = 0; m < 6; ++m) wp[m] = lp<u32x4>(L, L_WP)[(vw * 6 + m) * 64 + lane];
}
__device__ __forceinline__ void finish_o(LAS unsigned char* L, LAS u32* ob, int lane) {
  const LAS f32x4* rp = lp<f32x4>(L, L_RED) + (lane < 48 ? lane : 0);
  const f32x4 s = (rp[0] + rp[48]) + (rp[96] + rp[144]);
  if (lane < 48) ((LAS u32x2*)ob)[lane] = u32x2{pk_bf16(s.x, s.y), pk_bf16(s.z, s.w)};
  asm volatile("s_waitcnt lgkmcnt(0)" ::: "memory");
}
__device__ __forceinline__ f32x2 gemv16(const LAS u32* ob, const u32x4 (&wp)[6], int lane) {
  const LAS u32x4* op = (const LAS u32x4*)ob + (lane >> 3) * 3;
  const u32x4 o0 = op[0], o1 = op[1], o2 = op[2];
  float y[4] = {0.f, 0.f, 0.f, 0.f};
#pragma unroll
  for (int m = 0; m < 12; ++m) {
    const u32 o = m < 4 ? o0[m & 3] : m < 8 ? o1[m & 3] : o2[m & 3];
    y[2 * (m & 1)] = dot2(wp[m >> 1][2 * (m & 1)], o, y[2 * (m & 1)]); y[2 * (m & 1) + 1] = dot2(wp[m >> 1][2 * (m & 1) + 1], o, y[2 * (m & 1) + 1]);
  }
  float y0 = y[0] + y[2], y1 = y[1] + y[3];
  y0 += dpp<R8>(y0); y1 += dpp<R8>(y1);
  y0 = tsum16(y0, y0); y1 = tsum16(y1, y1);
  return f32x2{tsum32(y0, y0), tsum32(y1, y1)};
}

__global__ void __launch_bounds__(NTHR, 1) fused_block(Args a) {
  __shared__ __attribute__((aligned(16))) unsigned char lds_raw[LDS_BYTES];
  LAS unsigned char* L = (LAS unsigned char*)lds_raw;
  const int tid = threadIdx.x, lane = tid & 63, w = __builtin_amdgcn_readfirstlane(tid >> 6);
  const int b = blockIdx.x, xg = b & 7, j = b >> 3;
  const int r = lane >> 3, j8 = lane & 7;
  const int tj = j >> 3, c8 = j & 7;
  volatile LAS u32* FL = lp<u32>(L, L_FLAG);
  volatile LAS u32* dead = FL + F_DEAD;
  STAMP(0);
  const rsrc_t ws = mk(a.ws, WS_BYTES);
  const rsrc_t rx = mk(a.x, D * 2), rq = mk(a.wq, D * H * DH * 2);

  const float EPS = 1e-5f;
  u64 pc0; u32 clen;
  asm volatile("s_getpc_b64 %0\n.Lfb_begin:\n\ts_mov_b32 %1, .Lfb_end-.Lfb_begin" : "=s"(pc0), "=s"(clen));
  auto code_touch = [&](int piece) {
    const u32 v = __builtin_amdgcn_raw_buffer_load_b32(mk((const void*)pc0, clen), (u32)piece * 1024u + (u32)lane * 16u, 0, 0);
    asm volatile("" :: "v"(v));
  };
  u32 eoff = WS_E; asm volatile("" : "+v"(eoff));
  const u32 voff = (u32)r * 384u + (u32)j8 * 16u;
  const u32 sbase = (u32)xg * HEADB + (u32)j * 24576u + (u32)w * 6144u;
  auto step_off = [&](int n) -> u32 { return sbase + (u32)(n >> 4) * (8u * HEADB) + (u32)(n & 15) * (32u * 24576u); };
  u32x2 ev; u32 xme, T0, T1;
  auto make_tag = [&]() {
    u64 z = (((u64)ev.y << 32) | ev.x) + 0x9E3779B97F4A7C15ull;
    z = (z ^ (z >> 30)) * 0xBF58476D1CE4E5B9ull; z = (z ^ (z >> 27)) * 0x94D049BB133111EBull; z ^= z >> 31;
    T0 = __builtin_amdgcn_readfirstlane((u32)z) | 1u; T1 = __builtin_amdgcn_readfirstlane((u32)(z >> 32)) | 1u;
  };

  if (w == SVC) {
    ev = __builtin_amdgcn_raw_buffer_load_b64(ws, eoff, 0, AUX_SC1);
    xme = __builtin_amdgcn_raw_buffer_load_b32(rx, tid * 4, 0, 0);
    if (lane < 32) FL[lane] = 0u;
    if (CPF & 1) code_touch(j);
    make_tag();
    STAMP(1);
    bar();
    publish192(ws, lp<f32x2>(L, L_PART), WS_X0 + (u32)((xg * 32 + j) * 96) * 16u, lane, T0, T1);
    __builtin_amdgcn_s_sleep(QDELAY);
#pragma unroll 1
    for (int t = 0; t < 4; ++t) {
      float s[1][3];
      gather8<1, 4>(ws, L, WS_X0 + (u32)((xg * 32 + 8 * t) * 96) * 16u, lane, T0, T1, s);
#pragma unroll
      for (int pr = 0; pr < 3; ++pr) lp<u16>(L, L_Q)[t * 192 + (32 * pr + (lane & 31)) * 2 + (lane >> 5)] = (u16)pk_bf16(s[0][pr], 0.f);
      if (t == 0) { STAMP(2); bar(); }
      __builtin_amdgcn_s_sleep(64);
    }
    signal_add((LAS u32*)FL + F_Q, lane);
#pragma unroll 1
    for (int t = 0; t < 4; ++t) {
      wait_flag<8>(FL + F_K + t, 4u, dead);
      if (t == 0) {
#pragma unroll 1
        for (int vw = 0; vw < 5; ++vw) pack_wo(L, vw, lane);
      }
      LAS u32x4* sp = lp<u32x4>(L, L_S + ((lane >> 4) * 4 + t) * 512 + (lane & 15) * 32);
      const u32x4 s0 = sp[0], s1 = sp[1];
      float s[16];
#pragma unroll
      for (int e = 0; e < 4; ++e) { s[2 * e] = lo_f(s0[e]); s[2 * e + 1] = hi_f(s0[e]); s[8 + 2 * e] = lo_f(s1[e]); s[8 + 2 * e + 1] = hi_f(s1[e]); }
      float m = s[0];
#pragma unroll
      for (int e = 1; e < 16; ++e) m = fmaxf(m, s[e]);
      m = wave_max(m);
      const float L2E = 1.4426950408889634f;
      float l = 0.f;
#pragma unroll
      for (int e = 0; e < 16; ++e) l += __builtin_amdgcn_exp2f((s[e] - m) * L2E);
      l = wave_sum(l);
      put_rec(ws, lane == 0 ? WS_X1 + (u32)((t * 8 + xg) * 32 + j) * 128u : OOB, __float_as_uint(m), __float_as_uint(l), T0, T1);
      u32 off[1] = {WS_X1 + (u32)((t * 8 + xg) * 32 + (lane & 31)) * 128u}, d0[1], d1[1];
      __builtin_amdgcn_s_sleep(32);
      get_recs<1, 32>(ws, off, T0, T1, d0, d1, dead);
      const float mj = __uint_as_float(d0[0]), lj = __uint_as_float(d1[0]);
      const float M = wave_max(mj);
      float Ls = row_sum(lj * __builtin_amdgcn_exp2f((mj - M) * L2E));
      Ls = tsum16(Ls, Ls);
      Ls = __uint_as_float(__builtin_amdgcn_readfirstlane(__float_as_uint(Ls)));
      const float inv = 1.f / Ls;
      u32x4 p0, p1;
#pragma unroll
      for (int e = 0; e < 4; ++e) {
        p0[e] = pk_bf16(__builtin_amdgcn_exp2f((s[2 * e] - M) * L2E) * inv, __builtin_amdgcn_exp2f((s[2 * e + 1] - M) * L2E) * inv);
        p1[e] = pk_bf16(__builtin_amdgcn_exp2f((s[8 + 2 * e] - M) * L2E) * inv, __builtin_amdgcn_exp2f((s[8 + 2 * e + 1] - M) * L2E) * inv);
      }
      sp[0] = p0; sp[1] = p1;
      signal_add((LAS u32*)FL + F_P + t, lane);
    }
#pragma unroll 1
    for (int t = 0; t < 3; ++t) {
      wait_flag<8>(FL + F_V + t, 4u, dead);
      if (t == 2) STAMP(3);
      publish192(ws, lp<f32x2>(L, L_PART + t * 3072), WS_X2 + (u32)(((t * 8 + xg) * 32 + j) * 96) * 16u, lane, T0, T1);
      if (tj == t) {
        __builtin_amdgcn_s_sleep(DUTY0);
        {
          const int rc = lane & 15;
          u32 off[8], d0[8], d1[8];
#pragma unroll
          for (int n = 0; n < 8; ++n) off[n] = WS_X2 + (u32)(((t * 8 + xg) * 32 + 4 * n + (lane >> 4)) * 96 + 12 * c8 + (rc < 12 ? rc : 0)) * 16u;
          get_recs<8, 32>(ws, off, T0, T1, d0, d1, dead);
          if (t == 2) STAMP(4);
          float s0 = 0.f, s1 = 0.f;
#pragma unroll
          for (int n = 0; n < 8; ++n) { s0 += __uint_as_float(d0[n]); s1 += __uint_as_float(d1[n]); }
          s0 = tsum16(s0, s0); s1 = tsum16(s1, s1); s0 = tsum32(s0, s0); s1 = tsum32(s1, s1);
          const u32 op = pk_bf16(s0, s1), on = __float_as_uint(dpp<X1>(__uint_as_float(op)));
          put_rec(ws, (lane < 12 && !(lane & 1)) ? WS_XO + (u32)(xg * 24 + j) * 128u + (u32)lane * 8u : OOB, op, on, T0, T1);
        }
        __builtin_amdgcn_s_sleep(DUTY1);
        {
          const int l6 = (lane * 43) >> 8;
          u32 off[1] = {WS_XO + (lane < 48 ? (u32)(xg * 24 + 8 * t + l6) * 128u + (u32)(lane - 6 * l6) * 16u : (u32)(xg * 24 + 8 * t) * 128u)}, d0[1], d1[1];
          get_recs<1, 16>(ws, off, T0, T1, d0, d1, dead);
          if (lane < 48) lp<u32x2>(L, L_OB)[lane] = u32x2{d0[0], d1[0]};
          asm volatile("s_waitcnt lgkmcnt(0)" ::: "memory");
        }
        if (t == 2) STAMP(5);
#pragma unroll 1
        for (int vw = 0; vw < 5; ++vw) {
          u32x4 wp[6];
          load_wo(L, vw, lane, wp);
          f32x2 y = gemv16(lp<u32>(L, L_OB), wp, lane);
          if (t > 0) y += lp<f32x2>(L, L_Y)[8 * vw + j8];
          put_rec(ws, lane < 8 ? WS_XC + (u32)(xg * 24 + j) * 640u + (u32)(8 * vw + lane) * 16u : OOB, __float_as_uint(y.x), __float_as_uint(y.y), T0, T1);
        }
      }
      if (tj == t + 1) {
        __builtin_amdgcn_s_sleep(127); __builtin_amdgcn_s_sleep(DUTY2);
        u32 off[1] = {WS_XC + (u32)(xg * 24 + j - 8) * 640u + (u32)(lane < 40 ? lane : 0) * 16u}, d0[1], d1[1];
        get_recs<1, 32>(ws, off, T0, T1, d0, d1, dead);
        if (lane < 40) lp<u32x2>(L, L_Y)[lane] = u32x2{d0[0], d1[0]};
      }
      if (t == 2) STAMP(13);
    }
    if ((CPF & 2) && tj < 2) { wait_flag<8>(FL + F_BLK, 1u, dead); code_touch(2 * j); code_touch(2 * j + 1); }
  } else {
    u32 xs[5]; u16 xr[3]; u32x4 wqv[3][3]; u32x4 ring[RING][2];
#pragma unroll
    for (int m = 0; m < 5; ++m) xs[m] = __builtin_amdgcn_raw_buffer_load_b32(rx, (lane + 64 * m) * 4, 0, 0);
    {
      const u32 row = (u32)(80 * (j & 7) + 20 * w + r);
      const u32 o = row * (u32)(H * DH * 2) + (u32)(8 * (j >> 3) + xg) * 384u + (u32)j8 * 16u;
#pragma unroll
      for (int g = 0; g < 3; ++g) {
        const u32 og = (g < 2 || r < 4) ? o : OOB, ox = (g < 2 || r < 4) ? row * 2u : OOB;
#pragma unroll
        for (int i = 0; i < 3; ++i) wqv[g][i] = __builtin_amdgcn_raw_buffer_load_b128(rq, og + i * 128, g * 8 * (H * DH * 2), AUX_NT);
        xr[g] = __builtin_amdgcn_raw_buffer_load_b16(rx, ox, g * 16, 0);
      }
    }
    ev = __builtin_amdgcn_raw_buffer_load_b64(ws, eoff, 0, AUX_SC1);
    xme = __builtin_amdgcn_raw_buffer_load_b32(rx, tid * 4, 0, 0);
    SB();
    auto issue = [&](int slot, int n, int i) {
      const rsrc_t rs = mk(n < 64 ? a.k : a.v, n < 128 ? HEADB * H : 0u);
      const u32 so = step_off(n & 63);
      ring[slot][0] = __builtin_amdgcn_raw_buffer_load_b128(rs, voff + i * 128, so, AUX_NT);
      ring[slot][1] = __builtin_amdgcn_raw_buffer_load_b128(rs, voff + 3072 + i * 128, so, AUX_NT);
      SB();
    };
#pragma unroll
    for (int u = 0; u < RING; ++u) issue(u, u / 3, u % 3);
    float acc[24];
    {
      float s = 0.f;
#pragma unroll
      for (int m = 0; m < 5; ++m) s += lo_f(xs[m]) + hi_f(xs[m]);
      const float mean = wave_sum(s) * (1.f / D);
      float q2 = 0.f;
#pragma unroll
      for (int m = 0; m < 5; ++m) { const float d0 = lo_f(xs[m]) - mean, d1 = hi_f(xs[m]) - mean; q2 += d0 * d0 + d1 * d1; }
      const float var = rbf(wave_sum(q2) * (1.f / D));
      const float mb = rbf(mean), inv = __builtin_amdgcn_rcpf(rbf(__builtin_amdgcn_sqrtf(rbf(var + EPS))));
#pragma unroll
      for (int e = 0; e < 24; ++e) acc[e] = 0.f;
#pragma unroll
      for (int g = 0; g < 3; ++g) {
        const u32 xl = pk_bf16(rbf(lo_f(xr[g]) - mb) * inv, 0.f), xh = xl << 16;
#pragma unroll
        for (int i = 0; i < 3; ++i)
#pragma unroll
          for (int e = 0; e < 4; ++e) {
            acc[i * 8 + 2 * e] = dot2(wqv[g][i][e], xl, acc[i * 8 + 2 * e]);
            acc[i * 8 + 2 * e + 1] = dot2(wqv[g][i][e], xh, acc[i * 8 + 2 * e + 1]);
          }
      }
      flush24(acc, lp<float>(L, L_PART + w * 768), lane);
    }
    STAMP(1);
    bar();
    u32x4 wov[8];
    {
      const rsrc_t ro = mk(a.wo, H * DH * D * 2);
#pragma unroll
      for (int n = 0; n < 8; ++n) {
        const u32 c = (u32)(480 * w + 64 * n + lane), row = (c * 6554u) >> 16;
        const u32 oo = ((u32)(8 * tj + xg) * DH + row) * (D * 2) + 160u * c8 + (c - 10u * row) * 16u;
        wov[n] = __builtin_amdgcn_raw_buffer_load_b128(ro, (n < 7 || lane < 32) ? oo : OOB, 0, AUX_NT);
      }
    }
    SB();
    make_tag();
    bar();
    STAMP(2);
    u32 nxt = (u32)rt() * 16u;
#define PACE_STEP() do { u32 now_ = (u32)rt() * 16u; \
      while ((int)(nxt - now_) > 0) { __builtin_amdgcn_s_sleep(1); now_ = (u32)rt() * 16u; } \
      const u32 lo_ = now_ - PACE_BURST * 16u; if ((int)(lo_ - nxt) > 0) nxt = lo_; nxt += PACE; } while (0)
    LAS u32x4* sp = lp<u32x4>(L, L_S + w * 2048) + r;
    const float SCALE = 0.07216878364870322f;
    u32x4 q[3];
    auto k_block = [&](int bk, bool first) {
      u32x4 sc;
#pragma unroll
      for (int st = 0; st < 4; ++st) {
        PACE_STEP();
        float d[2] = {0.f, 0.f};
#pragma unroll
        for (int i = 0; i < 3; ++i) {
          const int u = 3 * st + i, slot = u % RING;
#pragma unroll
          for (int e = 0; e < 4; ++e) { d[0] = dot2(ring[slot][0][e], q[i][e], d[0]); d[1] = dot2(ring[slot][1][e], q[i][e], d[1]); }
          asm volatile("" : "+v"(d[0]), "+v"(d[1]));
          SB();
          issue(slot, 4 * bk + (u + RING) / 3, (u + RING) % 3);
        }
#pragma unroll
        for (int g = 0; g < 2; ++g) { d[g] += dpp<X1>(d[g]); d[g] += dpp<X2>(d[g]); d[g] += dpp<X7>(d[g]); }
        const u32 s1 = pk_bf16(d[0], d[1]);
        sc[st] = pk_bf16(lo_f(s1) * SCALE, hi_f(s1) * SCALE);
        if (first && st == 1) {
#pragma unroll
          for (int n = 0; n < 8; ++n) if (n < 7 || lane < 32) lp<u32x4>(L, L_WO)[480 * w + 64 * n + lane] = wov[n];
        }
      }
      if (j8 == 0) sp[bk * 8] = sc;
    };
    auto load_q = [&](int t) {
#pragma unroll
      for (int i = 0; i < 3; ++i) q[i] = lp<u32x4>(L, L_Q + t * 384)[j8 + 8 * i];
    };
    load_q(0);
    k_block(0, true);
#pragma unroll 1
    for (int bk = 1; bk < 16; ++bk) {
      if ((bk & 3) == 0) { signal_add((LAS u32*)FL + F_K + (bk >> 2) - 1, lane); wait_flag<1>(FL + F_Q, 1u, dead); load_q(bk >> 2); }
      k_block(bk, false);
    }
    signal_add((LAS u32*)FL + F_K + 3, lane);
    STAMP(3);
#pragma unroll 1
    for (int bk = 0; bk < 16; ++bk) {
      if ((bk & 3) == 0) {
        wait_flag<1>(FL + F_P + (bk >> 2), 1u, dead);
#pragma unroll
        for (int e = 0; e < 24; ++e) acc[e] = 0.f;
      }
      const u32x4 pp = sp[bk * 8];
      if (bk == CPB) FL[F_BLK] = 1u;
#pragma unroll
      for (int st = 0; st < 4; ++st) {
        if (3 * st + RING < 12 || bk < 15) PACE_STEP();
#pragma unroll
        for (int i = 0; i < 3; ++i) {
          const int u = 3 * st + i, slot = u % RING;
#pragma unroll
          for (int e = 0; e < 4; ++e) {
            acc[i * 8 + 2 * e] = dot2(zip_lo(ring[slot][0][e], ring[slot][1][e]), pp[st], acc[i * 8 + 2 * e]);
            acc[i * 8 + 2 * e + 1] = dot2(zip_hi(ring[slot][0][e], ring[slot][1][e]), pp[st], acc[i * 8 + 2 * e + 1]);
          }
#pragma unroll
          for (int e = 0; e < 8; ++e) asm volatile("" : "+v"(acc[i * 8 + e]));
          SB();
          issue(slot, 64 + 4 * bk + (u + RING) / 3, (u + RING) % 3);
        }
      }
      if ((bk & 3) == 3) {
        if (bk == 15) STAMP(4); else STAMP(14 + (bk >> 2));
        flush24(acc, lp<float>(L, L_PART + ((bk >> 2) * 4 + w) * 768), lane);
        if (bk < 15) signal_add((LAS u32*)FL + F_V + (bk >> 2), lane);
      }
    }
    {
      asm volatile("s_waitcnt lgkmcnt(0)" ::: "memory");
      u32 old = 0u;
      if (lane == 0) old = __hip_atomic_fetch_add((LAS u32*)FL + F_L, 1u, __ATOMIC_RELAXED, __HIP_MEMORY_SCOPE_WORKGROUP);
      if (__builtin_amdgcn_readfirstlane(old) == 3u) {
        asm volatile("" ::: "memory");
        publish192(ws, lp<f32x2>(L, L_PART + 3 * 3072), WS_X2 + (u32)(((3 * 8 + xg) * 32 + j) * 96) * 16u, lane, T0, T1);
        STAMP(6);
      }
      SB();
    }
    {
      const u32 og = r < 5 ? (u32)(20 * j + 10 * (w >> 1) + r) * (F * 2u) + 384u * xg + (u32)j8 * 16u : OOB;
      const u32 od = (lane & 15) < 12 ? (u32)(48 * j + 12 * w + (lane >> 4)) * (D * 2u) + ((160u * xg) & ~63u) + (u32)(lane & 15) * 16u : OOB;
      u32 v[9], keep = 0u;
#pragma unroll
      for (int n = 0; n < 6; ++n) v[n] = __builtin_amdgcn_raw_buffer_load_b32(mk((w & 1) ? a.wu : a.wg, D * F * 2), og + (n % 3) * 128, (n / 3) * 5 * (F * 2), 0);
#pragma unroll
      for (int n = 0; n < 3; ++n) v[6 + n] = __builtin_amdgcn_raw_buffer_load_b32(mk(a.wd, F * D * 2), od, n * 4 * (D * 2), 0);
      SB();
#pragma unroll
      for (int n = 0; n < 9; ++n) keep |= v[n];
      asm volatile("" :: "v"(keep));
    }
    __builtin_amdgcn_s_waitcnt(0x0F70);
    STAMP(5);
  }

  if (tj == 3) {
    u32x4 wp[6];
    load_wo(L, w, lane, wp);
#pragma unroll
    for (int e = 0; e < 6; ++e) asm volatile("" : "+v"(wp[e]));
    if (w != SVC) {
      __builtin_amdgcn_s_sleep(PRE);
      { u32 off[1] = {WS_X2 + (u32)(((3 * 8 + xg) * 32 + 8 * w + j8) * 96 + 95) * 16u}, d0[1], d1[1];
        get_recs<1, 2>(ws, off, T0, T1, d0, d1, dead); }
      gather_o<1, SLP_A>(ws, L, 3, xg, w, lane, T0, T1);
    }
    STAMP(7);
    bar();
    f32x2 y = lp<f32x2>(L, L_Y)[8 * w + j8];
    finish_o(L, lp<u32>(L, L_OB + w * 384), lane);
    y += gemv16(lp<u32>(L, L_OB + w * 384), wp, lane);
    put_rec(ws, WS_X3 + (u32)r * X3_REP + (u32)(xg * 320 + 40 * c8 + 8 * w + j8) * 16u, __float_as_uint(y.x), __float_as_uint(y.y), T0, T1);
    SB();
    STAMP(8);
    __builtin_amdgcn_s_barrier();
    SB();
    if (w < 3) {
      u32x4 da[8]; u32 db[8];
      const rsrc_t rd = mk(a.wd, F * D * 2);
      const u32 od = (u32)tid * (8u * D * 2u) + 160u * xg + 20u * c8;
#pragma unroll
      for (int n = 0; n < 8; ++n) {
        da[n] = __builtin_amdgcn_raw_buffer_load_b128(rd, od, n * D * 2, 0);
        db[n] = __builtin_amdgcn_raw_buffer_load_b32(rd, od + 16u, n * D * 2, 0);
      }
      u32 dp[4][10];
#pragma unroll
      for (int n = 0; n < 4; ++n) {
#pragma unroll
        for (int e = 0; e < 4; ++e) { dp[n][2 * e] = zip_lo(da[2 * n][e], da[2 * n + 1][e]); dp[n][2 * e + 1] = zip_hi(da[2 * n][e], da[2 * n + 1][e]); }
        dp[n][8] = zip_lo(db[2 * n], db[2 * n + 1]); dp[n][9] = zip_hi(db[2 * n], db[2 * n + 1]);
#pragma unroll
        for (int e = 0; e < 10; ++e) asm volatile("" : "+v"(dp[n][e]));
      }
      STAMP(10);
      u32 off[2] = {WS_X4 + (u32)xg * X4_REP + (u32)tid * 128u, WS_X4 + (u32)xg * X4_REP + (u32)tid * 128u + 16u}, d0[2], d1[2];
      get_recs<2, SLP_C>(ws, off, T0, T1, d0, d1, dead);
      STAMP(11);
      float c[10], c5[5];
#pragma unroll
      for (int e = 0; e < 10; ++e) c[e] = dot2(dp[3][e], d1[1], dot2(dp[2][e], d0[1], dot2(dp[1][e], d1[0], dot2(dp[0][e], d0[0], 0.f))));
#pragma unroll
      for (int e = 0; e < 5; ++e) c5[e] = tsum32(c[e], c[e + 5]);
      const float v0 = row_sum(tsum16(c5[0], c5[1])), v1 = row_sum(tsum16(c5[2], c5[3])), v2 = row_sum(tsum16(c5[4], c5[4]));
      if ((lane & 15) == 0) {
        LAS float* dn = lp<float>(L, L_DN) + w * 16 + 5 * (lane >> 5);
        const int b4 = (lane >> 4) & 1;
        dn[b4] = v0; dn[2 + b4] = v1; dn[4] = v2;
      }
    } else if (w == 3) {
      const int cp = lane & 7;
      u32 off[1] = {WS_X3 + (u32)xg * X3_REP + (u32)(r * 320 + 40 * xg + 5 * c8 + (cp < 5 ? cp : 0)) * 16u}, d0[1], d1[1];
      const u32 xv = __builtin_amdgcn_raw_buffer_load_b32(rx, (40 * xg + 5 * c8 + (cp < 5 ? cp : 0)) * 4, 0, 0);
      __builtin_amdgcn_s_sleep(PRE);
      get_recs<1, 8>(ws, off, T0, T1, d0, d1, dead);
      float s0 = __uint_as_float(d0[0]), s1 = __uint_as_float(d1[0]);
      s0 = tsum32(s0, s0); s1 = tsum32(s1, s1); s0 = tsum16(s0, s0); s1 = tsum16(s1, s1); s0 += dpp<R8>(s0); s1 += dpp<R8>(s1);
      const u32 at = pk_bf16(s0, s1);
      if (lane < 5) lp<u32>(L, L_H)[lane] = pk_bf16(lo_f(xv) + lo_f(at), hi_f(xv) + hi_f(at));
    }
    bar();
    if (tid < 10) {
      const float s = rbf((lp<float>(L, L_DN)[tid] + lp<float>(L, L_DN)[16 + tid]) + lp<float>(L, L_DN)[32 + tid]);
      a.out[80 * xg + 10 * c8 + tid] = (u16)pk_bf16(lo_f(lp<u16>(L, L_H)[tid]) + s, 0.f);
    }
  } else {
    bar();
    u32 gp[16];
    {
      const rsrc_t rg = mk(a.wg, D * F * 2), ru = mk(a.wu, D * F * 2);
      const u32 og = (u32)tid * (2u * F * 2u) + 384u * xg + 16u * j;
      const u32x4 A = __builtin_amdgcn_raw_buffer_load_b128(rg, og, 0, 0), B = __builtin_amdgcn_raw_buffer_load_b128(rg, og, F * 2, 0);
      const u32x4 C = __builtin_amdgcn_raw_buffer_load_b128(ru, og, 0, 0), E = __builtin_amdgcn_raw_buffer_load_b128(ru, og, F * 2, 0);
#pragma unroll
      for (int e = 0; e < 4; ++e) {
        gp[2 * e] = zip_lo(A[e], B[e]); gp[2 * e + 1] = zip_hi(A[e], B[e]);
        gp[8 + 2 * e] = zip_lo(C[e], E[e]); gp[8 + 2 * e + 1] = zip_hi(C[e], E[e]);
      }
#pragma unroll
      for (int e = 0; e < 16; ++e) asm volatile("" : "+v"(gp[e]));
    }
    STAMP(8);
    if (w == SVC) {
      u32 off[1] = {WS_X3 + (u32)xg * X3_REP + (u32)(r * 320 + 40 * j8 + 39) * 16u}, d0[1], d1[1];
      get_recs<1, 4>(ws, off, T0, T1, d0, d1, dead);
    }
    bar();
    u32 hp;
    {
      u32 off[8], d0[8], d1[8];
#pragma unroll
      for (int k = 0; k < 8; ++k) off[k] = WS_X3 + (u32)xg * X3_REP + (u32)(k * 320 + tid) * 16u;
      get_recs<8, SLP_B>(ws, off, T0, T1, d0, d1, dead);
      auto sum8 = [](const u32 (&d)[8]) {
        float t[4];
#pragma unroll
        for (int k = 0; k < 4; ++k) t[k] = __uint_as_float(d[k]) + __uint_as_float(d[k + 4]);
        return (t[0] + t[2]) + (t[1] + t[3]);
      };
      const u32 at = pk_bf16(sum8(d0), sum8(d1));
      hp = pk_bf16(lo_f(xme) + lo_f(at), hi_f(xme) + hi_f(at));
    }
    STAMP(9);
    const float h0 = lo_f(hp), h1 = hi_f(hp);
    {
      const float mw = wave_sum(h0 + h1) * (1.f / 128.f);
      const float m2 = wave_sum((h0 - mw) * (h0 - mw) + (h1 - mw) * (h1 - mw));
      if (lane == 0) lp<f32x2>(L, L_ST)[w] = f32x2{mw, m2};
    }
    bar();
    if (b == 0 && tid == 0) {
      const u64 e1 = (((u64)ev.y << 32) | ev.x) + 1ull;
      __builtin_amdgcn_raw_buffer_store_b64(u32x2{(u32)e1, (u32)(e1 >> 32)}, ws, WS_E, 0, AUX_SC1);
    }
    f32x2 st[5];
#pragma unroll
    for (int ww = 0; ww < 5; ++ww) st[ww] = lp<f32x2>(L, L_ST)[ww];
    const float mean = (((st[0].x + st[1].x) + (st[2].x + st[3].x)) + st[4].x) * 0.2f;
    float m2 = 0.f;
#pragma unroll
    for (int ww = 0; ww < 5; ++ww) m2 += st[ww].y + 128.f * (st[ww].x - mean) * (st[ww].x - mean);
    const float var = rbf(m2 * (1.f / D));
    const float mb = rbf(mean), inv = __builtin_amdgcn_rcpf(rbf(__builtin_amdgcn_sqrtf(rbf(var + EPS))));
    const u32 dd = pk_bf16(h0 - mb, h1 - mb);
    const u32 hn = pk_bf16(lo_f(dd) * inv, hi_f(dd) * inv);
    float g[16];
#pragma unroll
    for (int e = 0; e < 16; ++e) g[e] = dot2(gp[e], hn, 0.f);
    float g8[8], g4[4], g2[2];
#pragma unroll
    for (int e = 0; e < 8; ++e) g8[e] = tsum32(g[e], g[e + 8]);
#pragma unroll
    for (int e = 0; e < 4; ++e) g4[e] = tsum16(g8[e], g8[e + 4]);
    const bool b3 = (lane & 8) != 0, b2 = (lane & 4) != 0;
#pragma unroll
    for (int e = 0; e < 2; ++e) g2[e] = (b3 ? g4[e + 2] : g4[e]) + dpp<R8>(b3 ? g4[e] : g4[e + 2]);
    float g1 = (b2 ? g2[1] : g2[0]) + dpp<X7>(b2 ? g2[0] : g2[1]);
    g1 += dpp<X2>(g1); g1 += dpp<X1>(g1);
    if ((lane & 3) == 0) lp<float>(L, L_GU)[w * 16 + (lane >> 2)] = g1;
    bar();
    if (w == 0) {
      float gs = 0.f, us = 0.f;
#pragma unroll
      for (int ww = 0; ww < 5; ++ww) { gs += lp<float>(L, L_GU)[ww * 16 + j8]; us += lp<float>(L, L_GU)[ww * 16 + 8 + j8]; }
      const u32 gu = pk_bf16(gs, us);
      const float sg = rbf(__builtin_amdgcn_rcpf(1.f + __builtin_amdgcn_exp2f(-1.4426950408889634f * lo_f(gu))));
      const float f = sg * hi_f(gu);
      const u32 d0 = pk_bf16(dpp<0x00>(f), dpp<0x55>(f)), d1 = pk_bf16(dpp<0xAA>(f), dpp<0xFF>(f));
      put_rec(ws, (lane & 3) == 0 ? WS_X4 + (u32)r * X4_REP + (u32)(xg * 24 + j) * 128u + (u32)(lane & 4) * 4u : OOB, d0, d1, T0, T1);
    }
    STAMP(10);
  }
  STAMP(12);
  asm volatile(".Lfb_end:");
}
}

extern "C" void kernel_launch(void* const* d_in, const int* in_sizes, int n_in, void* d_out, int out_size, void* d_ws, size_t ws_size,
                              hipStream_t stream) {
  Args a;
  a.x = (const u16*)d_in[0]; a.k = (const u16*)d_in[1]; a.v = (const u16*)d_in[2]; a.wq = (const u16*)d_in[3]; a.wo = (const u16*)d_in[4];
  a.wg = (const u16*)d_in[6]; a.wu = (const u16*)d_in[7]; a.wd = (const u16*)d_in[8];
  a.out = (u16*)d_out; a.ws = (unsigned char*)d_ws;
  hipLaunchKernelGGL(fused_block, dim3(256), dim3(NTHR), 0, stream, a);
}
```

```cpp
#include <hip/hip_runtime.h>
#include <hip/hip_bf16.h>
#include <stdint.h>

#define STAMPS 0
#define PACE 1340
#define RING 6
#define PFT 1
#define BG 2
#define SENT_A 1
#define SENT_B 1
#define SLP_A 0
#define SLP_B 0
#define SLP_C 2
#define NOPF 0
#define DUTY0 40
#define DUTY1 10
#define DUTY2 32
#define PFWAIT 1
#define CPF 3
#define CPB 14
#define TCH 3
#define QDELAY 32
#define PRE 10
#define PACE_BURST 5000

typedef unsigned u32;
typedef unsigned long long u64;
typedef unsigned short u16;
typedef u32 u32x2 __attribute__((ext_vector_type(2)));
typedef u32 u32x3 __attribute__((ext_vector_type(3)));
typedef u32 u32x4 __attribute__((ext_vector_type(4)));
typedef float f32x2 __attribute__((ext_vector_type(2)));
typedef float f32x4 __attribute__((ext_vector_type(4)));
typedef __bf16 bf16x2 __attribute__((ext_vector_type(2)));
typedef __amdgpu_buffer_rsrc_t rsrc_t;
#define LAS __attribute__((address_space(3)))

namespace {
constexpr int D = 640, H = 32, SK = 32768, DH = 192, F = 1536;
constexpr u32 HEADB = (u32)SK * DH * 2;
constexpr int NTHR = 320, SVC = 4;
constexpr u32 OOB = 0x80000000u;
constexpr int AUX_NT = 2, AUX_SC1 = 16;
constexpr u32 WS_E = 0, WS_X1 = 4096, WS_X2 = WS_X1 + 4 * 8 * 32 * 128, WS_X0 = WS_X2 + 4 * 8 * 32 * 1536, WS_XC = WS_X0 + 8 * 32 * 1536,
              WS_XO = WS_XC + 8 * 24 * 640, WS_X3 = WS_XO + 8 * 24 * 128, X3_REP = 8 * 320 * 16, WS_X4 = WS_X3 + 8 * X3_REP, X4_REP = 192 * 128, WS_BYTES = WS_X4 + 8 * X4_REP;
constexpr u32 WS_STAMP = 4u << 20;
static_assert(WS_BYTES <= WS_STAMP && WS_XC % 128 == 0 && WS_X3 % 128 == 0 && WS_X4 % 128 == 0, "ws");
constexpr int L_Q = 0, L_Y = 1536, L_FLAG = 2048, L_ST = 2176, L_GU = 2240, L_DN = 2560, L_H = 2752, L_OB = 3072, L_RED = 5120,
              L_S = 8192, L_PART = 16384, L_WO = 28672, L_WP = 59392, LDS_BYTES = L_WP + 30720;
constexpr int F_K = 0, F_P = 4, F_V = 8, F_O = 12, F_DEAD = 13, F_Q = 14, F_L = 15, F_BLK = 16;
constexpr u32 SPIN_G = 1u << 17, SPIN_L = 1u << 21;

struct Args {
  const u16 *x, *k, *v, *wq, *wo, *wg, *wu, *wd;
  u16* out;
  unsigned char* ws;
};

__device__ __forceinline__ float dot2(u32 a, u32 b, float c) {
  return __builtin_amdgcn_fdot2_f32_bf16(__builtin_bit_cast(bf16x2, a), __builtin_bit_cast(bf16x2, b), c, false);
}
__device__ __forceinline__ u32 pk_bf16(float lo, float hi) {
  bf16x2 r = {(__bf16)lo, (__bf16)hi};
  return __builtin_bit_cast(u32, r);
}
__device__ __forceinline__ float lo_f(u32 a) { return __uint_as_float(a << 16); }
__device__ __forceinline__ float hi_f(u32 a) { return __uint_as_float(a & 0xffff0000u); }
__device__ __forceinline__ float rbf(float a) { return lo_f(pk_bf16(a, a)); }
__device__ __forceinline__ u32 zip_lo(u32 a, u32 b) { return __builtin_amdgcn_perm(b, a, 0x05040100u); }
__device__ __forceinline__ u32 zip_hi(u32 a, u32 b) { return __builtin_amdgcn_perm(b, a, 0x07060302u); }
template <int CTRL> __device__ __forceinline__ float dpp(float x) {
  return __builtin_bit_cast(float, __builtin_amdgcn_mov_dpp(__builtin_bit_cast(int, x), CTRL, 0xf, 0xf, true));
}
constexpr int X1 = 0xB1, X2 = 0x4E, X7 = 0x141, R1 = 0x121, R2 = 0x122, R4 = 0x124, R8 = 0x128;
__device__ __forceinline__ float tsum32(float a, float b) {
  auto s = __builtin_amdgcn_permlane32_swap(__float_as_uint(a), __float_as_uint(b), false, false);
  return __uint_as_float(s[0]) + __uint_as_float(s[1]);
}
__device__ __forceinline__ float tsum16(float a, float b) {
  auto s = __builtin_amdgcn_permlane16_swap(__float_as_uint(a), __float_as_uint(b), false, false);
  return __uint_as_float(s[0]) + __uint_as_float(s[1]);
}
__device__ __forceinline__ float row_sum(float x) { x += dpp<R8>(x); x += dpp<X7>(x); x += dpp<X2>(x); x += dpp<X1>(x); return x; }
__device__ __forceinline__ float row_max(float x) {
  x = fmaxf(x, dpp<R8>(x)); x = fmaxf(x, dpp<X7>(x)); x = fmaxf(x, dpp<X2>(x)); x = fmaxf(x, dpp<X1>(x)); return x;
}
__device__ __forceinline__ float wave_sum(float x) { x = row_sum(x); x = tsum16(x, x); return tsum32(x, x); }
__device__ __forceinline__ float wave_max(float x) {
  x = row_max(x);
  auto s = __builtin_amdgcn_permlane16_swap(__float_as_uint(x), __float_as_uint(x), false, false);
  x = fmaxf(__uint_as_float(s[0]), __uint_as_float(s[1]));
  auto t = __builtin_amdgcn_permlane32_swap(__float_as_uint(x), __float_as_uint(x), false, false);
  return fmaxf(__uint_as_float(t[0]), __uint_as_float(t[1]));
}
__device__ __forceinline__ rsrc_t mk(const void* p, u32 bytes) { return __builtin_amdgcn_make_buffer_rsrc((void*)p, 0, bytes, 0x00020000); }
__device__ __forceinline__ void bar() {
  asm volatile("s_waitcnt lgkmcnt(0)" ::: "memory");
  __builtin_amdgcn_s_barrier();
  asm volatile("" ::: "memory");
}
__device__ __forceinline__ void touch2(const void* p, const void* q) {
  u32 d0, d1;
  asm volatile("s_load_dword %0, %2, 0x0\n\ts_load_dword %1, %3, 0x0\n\ts_waitcnt lgkmcnt(0)" : "=&s"(d0), "=&s"(d1) : "s"(p), "s"(q) : "memory");
}
__device__ __forceinline__ u64 rt() { return __builtin_amdgcn_s_memrealtime(); }
#define SB() __builtin_amdgcn_sched_barrier(0)

template <typename T> __device__ __forceinline__ LAS T* lp(LAS unsigned char* l, int off) { return (LAS T*)(l + off); }

__device__ __forceinline__ void put_rec(rsrc_t ws, u32 off, u32 d0, u32 d1, u32 T0, u32 T1) {
  __builtin_amdgcn_raw_buffer_store_b128(u32x4{d0, d1, d0 ^ T0, d1 ^ T1}, ws, off, 0, AUX_SC1);
}
template <int N, int SLEEP>
__device__ __forceinline__ void get_recs(rsrc_t ws, const u32 (&off)[N], u32 T0, u32 T1, u32 (&d0)[N], u32 (&d1)[N], volatile LAS u32* dead) {
#pragma unroll 1
  for (u32 spin = 0;; ++spin) {
    asm volatile("" ::: "memory");
    u32x4 t[N];
#pragma unroll
    for (int k = 0; k < N; ++k) t[k] = __builtin_amdgcn_raw_buffer_load_b128(ws, off[k], 0, AUX_SC1);
    SB();
    u32 bad = 0u;
#pragma unroll
    for (int k = 0; k < N; ++k) { bad |= (t[k].x ^ t[k].z ^ T0) | (t[k].y ^ t[k].w ^ T1); d0[k] = t[k].x; d1[k] = t[k].y; }
    if (!__builtin_amdgcn_ballot_w64(bad != 0u)) break;
    if (SLEEP) __builtin_amdgcn_s_sleep(SLEEP);
    if ((spin & 255u) == 255u) { if (*dead) break; if (spin > SPIN_G) { *dead = 1u; break; } }
  }
}
template <int SLEEP>
__device__ __forceinline__ void wait_flag(volatile LAS u32* f, u32 want, volatile LAS u32* dead) {
  for (u32 spin = 0; (u32)__builtin_amdgcn_readfirstlane(*f) < want; ++spin) {
    if (SLEEP) __builtin_amdgcn_s_sleep(SLEEP);
    if ((spin & 255u) == 255u) { if (*dead) break; if (spin > SPIN_L) { *dead = 1u; break; } }
  }
  asm volatile("" ::: "memory");
}
__device__ __forceinline__ void signal_add(LAS u32* f, int lane) {
  asm volatile("s_waitcnt lgkmcnt(0)" ::: "memory");
  if (lane == 0) __hip_atomic_fetch_add(f, 1u, __ATOMIC_RELAXED, __HIP_MEMORY_SCOPE_WORKGROUP);
}

#define STAMP(i) do { } while (0)

template <int G, int SLEEP>
__device__ __forceinline__ void gather8(rsrc_t ws, LAS unsigned char* L, u32 first, int lane, u32 T0, u32 T1, float (&s)[G][3]) {
  u32 off[12 * G], d0[12 * G], d1[12 * G];
  const u32 base = first + (u32)((lane >> 5) * 96 + (lane & 31)) * 16u;
#pragma unroll
  for (int n = 0; n < 4 * G; ++n)
#pragma unroll
    for (int pr = 0; pr < 3; ++pr) off[n * 3 + pr] = base + (u32)(2 * n * 96 + 32 * pr) * 16u;
  get_recs<12 * G, SLEEP>(ws, off, T0, T1, d0, d1, lp<u32>(L, L_FLAG) + F_DEAD);
#pragma unroll
  for (int g = 0; g < G; ++g)
#pragma unroll
    for (int pr = 0; pr < 3; ++pr) {
      const int k = 12 * g + pr;
      float a = (__uint_as_float(d0[k]) + __uint_as_float(d0[k + 3])) + (__uint_as_float(d0[k + 6]) + __uint_as_float(d0[k + 9]));
      float b = (__uint_as_float(d1[k]) + __uint_as_float(d1[k + 3])) + (__uint_as_float(d1[k + 6]) + __uint_as_float(d1[k + 9]));
      s[g][pr] = tsum32(a, b);
    }
}
template <int G, int SLEEP>
__device__ __forceinline__ void gather_o(rsrc_t ws, LAS unsigned char* L, int t, int xg, int vw, int lane, u32 T0, u32 T1) {
  float s[G][3];
  gather8<G, SLEEP>(ws, L, WS_X2 + (u32)(((t * 8 + xg) * 32 + 8 * vw) * 96) * 16u, lane, T0, T1, s);
#pragma unroll
  for (int g = 0; g < G; ++g)
#pragma unroll
    for (int pr = 0; pr < 3; ++pr) lp<float>(L, L_RED)[(((vw + g) * 3 + pr) * 32 + (lane & 31)) * 2 + (lane >> 5)] = s[g][pr];
}
__device__ __forceinline__ void flush24(const float (&acc)[24], LAS float* dst, int lane) {
#pragma unroll
  for (int i = 0; i < 3; ++i) {
    float s1[4], s2[2];
#pragma unroll
    for (int e = 0; e < 4; ++e) s1[e] = tsum32(acc[i * 8 + e], acc[i * 8 + e + 4]);
#pragma unroll
    for (int e = 0; e < 2; ++e) s2[e] = tsum16(s1[e], s1[e + 2]);
    const bool b3 = (lane & 8) != 0;
    const float keep = b3 ? s2[1] : s2[0], send = b3 ? s2[0] : s2[1];
    dst[64 * i + 8 * (lane & 7) + (lane >> 3)] = keep + dpp<R8>(send);
  }
}
__device__ __forceinline__ void publish192(rsrc_t ws, const LAS f32x2* src, u32 first, int lane, u32 T0, u32 T1) {
  const LAS f32x2* p0 = src + lane; const LAS f32x2* p1 = src + 64 + (lane & 31);
  const f32x2 s0 = (p0[0] + p0[96]) + (p0[192] + p0[288]), s1 = (p1[0] + p1[96]) + (p1[192] + p1[288]);
  put_rec(ws, first + (u32)lane * 16u, __float_as_uint(s0.x), __float_as_uint(s0.y), T0, T1);
  put_rec(ws, lane < 32 ? first + 1024u + (u32)lane * 16u : OOB, __float_as_uint(s1.x), __float_as_uint(s1.y), T0, T1);
}
__device__ __forceinline__ void pack_wo(LAS unsigned char* L, int vw, int lane) {
  const LAS u32* src = lp<u32>(L, L_WO) + (lane >> 3) * 960 + 8 * vw + (lane & 7);
#pragma unroll
  for (int m = 0; m < 12; m += 2) {
    const u32 A = src[80 * m], B = src[80 * m + 40], C = src[80 * m + 80], E = src[80 * m + 120];
    lp<u32x4>(L, L_WP)[(vw * 6 + m / 2) * 64 + lane] = u32x4{zip_lo(A, B), zip_hi(A, B), zip_lo(C, E), zip_hi(C, E)};
  }
}
__device__ __forceinline__ void load_wo(LAS unsigned char* L, int vw, int lane, u32x4 (&wp)[6]) {
#pragma unroll
  for (int m = 0; m < 6; ++m) wp[m] = lp<u32x4>(L, L_WP)[(vw * 6 + m) * 64 + lane];
}
__device__ __forceinline__ void finish_o(LAS unsigned char* L, LAS u32* ob, int lane) {
  const LAS f32x4* rp = lp<f32x4>(L, L_RED) + (lane < 48 ? lane : 0);
  const f32x4 s = (rp[0] + rp[48]) + (rp[96] + rp[144]);
  if (lane < 48) ((LAS u32x2*)ob)[lane] = u32x2{pk_bf16(s.x, s.y), pk_bf16(s.z, s.w)};
  asm volatile("s_waitcnt lgkmcnt(0)" ::: "memory");
}
__device__ __forceinline__ f32x2 gemv16(const LAS u32* ob, const u32x4 (&wp)[6], int lane) {
  const LAS u32x4* op = (const LAS u32x4*)ob + (lane >> 3) * 3;
  const u32x4 o0 = op[0], o1 = op[1], o2 = op[2];
  float y[4] = {0.f, 0.f, 0.f, 0.f};
#pragma unroll
  for (int m = 0; m < 12; ++m) {
    const u32 o = m < 4 ? o0[m & 3] : m < 8 ? o1[m & 3] : o2[m & 3];
    y[2 * (m & 1)] = dot2(wp[m >> 1][2 * (m & 1)], o, y[2 * (m & 1)]); y[2 * (m & 1) + 1] = dot2(wp[m >> 1][2 * (m & 1) + 1], o, y[2 * (m & 1) + 1]);
  }
  float y0 = y[0] + y[2], y1 = y[1] + y[3];
  y0 += dpp<R8>(y0); y1 += dpp<R8>(y1);
  y0 = tsum16(y0, y0); y1 = tsum16(y1, y1);
  return f32x2{tsum32(y0, y0), tsum32(y1, y1)};
}

__global__ void __launch_bounds__(NTHR, 1) fused_block(Args a) {
  __shared__ __attribute__((aligned(16))) unsigned char lds_raw[LDS_BYTES];
  LAS unsigned char* L = (LAS unsigned char*)lds_raw;
  const int tid = threadIdx.x, lane = tid & 63, w = __builtin_amdgcn_readfirstlane(tid >> 6);
  const int b = blockIdx.x, xg = b & 7, j = b >> 3;
  const int r = lane >> 3, j8 = lane & 7;
  const int tj = j >> 3, c8 = j & 7;
  volatile LAS u32* FL = lp<u32>(L, L_FLAG);
  volatile LAS u32* dead = FL + F_DEAD;
  STAMP(0);
  const rsrc_t ws = mk(a.ws, WS_BYTES);
  const rsrc_t rx = mk(a.x, D * 2), rq = mk(a.wq, D * H * DH * 2);

  const float EPS = 1e-5f;
  u64 pc0; u32 clen;
  asm volatile("s_getpc_b64 %0\n.Lfb_begin:\n\ts_mov_b32 %1, .Lfb_end-.Lfb_begin" : "=s"(pc0), "=s"(clen));
  auto code_touch = [&](int piece) {
    const u32 v = __builtin_amdgcn_raw_buffer_load_b32(mk((const void*)pc0, clen), (u32)piece * 1024u + (u32)lane * 16u, 0, 0);
    asm volatile("" :: "v"(v));
  };
  u32 eoff = WS_E; asm volatile("" : "+v"(eoff));
  const u32 voff = (u32)r * 384u + (u32)j8 * 16u;
  const u32 sbase = (u32)xg * HEADB + (u32)j * 24576u + (u32)w * 6144u;
  auto step_off = [&](int n) -> u32 { return sbase + (u32)(n >> 4) * (8u * HEADB) + (u32)(n & 15) * (32u * 24576u); };
  u32x2 ev; u32 xme, T0, T1;
  auto make_tag = [&]() {
    u64 z = (((u64)ev.y << 32) | ev.x) + 0x9E3779B97F4A7C15ull;
    z = (z ^ (z >> 30)) * 0xBF58476D1CE4E5B9ull; z = (z ^ (z >> 27)) * 0x94D049BB133111EBull; z ^= z >> 31;
    T0 = __builtin_amdgcn_readfirstlane((u32)z) | 1u; T1 = __builtin_amdgcn_readfirstlane((u32)(z >> 32)) | 1u;
  };

  if (w == SVC) {
    ev = __builtin_amdgcn_raw_buffer_load_b64(ws, eoff, 0, AUX_SC1);
    xme = __builtin_amdgcn_raw_buffer_load_b32(rx, tid * 4, 0, 0);
    if (lane < 32) FL[lane] = 0u;
    if (CPF & 1) code_touch(j);
    if ((TCH & 1) && j >= 26) { const char* p = (const char*)a.k + (size_t)xg * HEADB + (size_t)(j - 25) * 2097152 - 64; touch2(p, p); }
    make_tag();
    STAMP(1);
    bar();
    publish192(ws, lp<f32x2>(L, L_PART), WS_X0 + (u32)((xg * 32 + j) * 96) * 16u, lane, T0, T1);
    __builtin_amdgcn_s_sleep(QDELAY);
#pragma unroll 1
    for (int t = 0; t < 4; ++t) {
      float s[1][3];
      gather8<1, 4>(ws, L, WS_X0 + (u32)((xg * 32 + 8 * t) * 96) * 16u, lane, T0, T1, s);
#pragma unroll
      for (int pr = 0; pr < 3; ++pr) lp<u16>(L, L_Q)[t * 192 + (32 * pr + (lane & 31)) * 2 + (lane >> 5)] = (u16)pk_bf16(s[0][pr], 0.f);
      if (t == 0) {
        STAMP(2); bar();
        if (TCH & 2) {
          auto at = [&](int n) -> const char* {
            if (n < 49) { const int sl = n / 7 + 1, pt = n % 7;
              return (const char*)(sl < 4 ? a.k : a.v) + (size_t)(8 * (sl & 3) + xg) * HEADB + (pt ? (size_t)pt * 2097152 - 64 : 0); }
            const int m = n - 49, e = (m & 1) * (D * F * 2 - 64);
            return m < 2 ? (const char*)a.wg + e : m < 4 ? (const char*)a.wu + e : m < 6 ? (const char*)a.wd + e : m < 8 ? (const char*)a.out : (const char*)a.ws + (m & 1) * (WS_BYTES - 64);
          };
          touch2(at(j), at(j + 32));
        }
      }
      __builtin_amdgcn_s_sleep(64);
    }
    signal_add((LAS u32*)FL + F_Q, lane);
#pragma unroll 1
    for (int t = 0; t < 4; ++t) {
      wait_flag<8>(FL + F_K + t, 4u, dead);
      if (t == 0) {
#pragma unroll 1
        for (int vw = 0; vw < 5; ++vw) pack_wo(L, vw, lane);
      }
      LAS u32x4* sp = lp<u32x4>(L, L_S + ((lane >> 4) * 4 + t) * 512 + (lane & 15) * 32);
      const u32x4 s0 = sp[0], s1 = sp[1];
      float s[16];
#pragma unroll
      for (int e = 0; e < 4; ++e) { s[2 * e] = lo_f(s0[e]); s[2 * e + 1] = hi_f(s0[e]); s[8 + 2 * e] = lo_f(s1[e]); s[8 + 2 * e + 1] = hi_f(s1[e]); }
      float m = s[0];
#pragma unroll
      for (int e = 1; e < 16; ++e) m = fmaxf(m, s[e]);
      m = wave_max(m);
      const float L2E = 1.4426950408889634f;
      float l = 0.f;
#pragma unroll
      for (int e = 0; e < 16; ++e) l += __builtin_amdgcn_exp2f((s[e] - m) * L2E);
      l = wave_sum(l);
      put_rec(ws, lane == 0 ? WS_X1 + (u32)((t * 8 + xg) * 32 + j) * 128u : OOB, __float_as_uint(m), __float_as_uint(l), T0, T1);
      u32 off[1] = {WS_X1 + (u32)((t * 8 + xg) * 32 + (lane & 31)) * 128u}, d0[1], d1[1];
      __builtin_amdgcn_s_sleep(32);
      get_recs<1, 32>(ws, off, T0, T1, d0, d1, dead);
      const float mj = __uint_as_float(d0[0]), lj = __uint_as_float(d1[0]);
      const float M = wave_max(mj);
      float Ls = row_sum(lj * __builtin_amdgcn_exp2f((mj - M) * L2E));
      Ls = tsum16(Ls, Ls);
      Ls = __uint_as_float(__builtin_amdgcn_readfirstlane(__float_as_uint(Ls)));
      const float inv = 1.f / Ls;
      u32x4 p0, p1;
#pragma unroll
      for (int e = 0; e < 4; ++e) {
        p0[e] = pk_bf16(__builtin_amdgcn_exp2f((s[2 * e] - M) * L2E) * inv, __builtin_amdgcn_exp2f((s[2 * e + 1] - M) * L2E) * inv);
        p1[e] = pk_bf16(__builtin_amdgcn_exp2f((s[8 + 2 * e] - M) * L2E) * inv, __builtin_amdgcn_exp2f((s[8 + 2 * e + 1] - M) * L2E) * inv);
      }
      sp[0] = p0; sp[1] = p1;
      signal_add((LAS u32*)FL + F_P + t, lane);
    }
#pragma unroll 1
    for (int t = 0; t < 3; ++t) {
      wait_flag<8>(FL + F_V + t, 4u, dead);
      if (t == 2) STAMP(3);
      publish192(ws, lp<f32x2>(L, L_PART + t * 3072), WS_X2 + (u32)(((t * 8 + xg) * 32 + j) * 96) * 16u, lane, T0, T1);
      if (tj == t) {
        __builtin_amdgcn_s_sleep(DUTY0);
        {
          const int rc = lane & 15;
          u32 off[8], d0[8], d1[8];
#pragma unroll
          for (int n = 0; n < 8; ++n) off[n] = WS_X2 + (u32)(((t * 8 + xg) * 32 + 4 * n + (lane >> 4)) * 96 + 12 * c8 + (rc < 12 ? rc : 0)) * 16u;
          get_recs<8, 32>(ws, off, T0, T1, d0, d1, dead);
          if (t == 2) STAMP(4);
          float s0 = 0.f, s1 = 0.f;
#pragma unroll
          for (int n = 0; n < 8; ++n) { s0 += __uint_as_float(d0[n]); s1 += __uint_as_float(d1[n]); }
          s0 = tsum16(s0, s0); s1 = tsum16(s1, s1); s0 = tsum32(s0, s0); s1 = tsum32(s1, s1);
          const u32 op = pk_bf16(s0, s1), on = __float_as_uint(dpp<X1>(__uint_as_float(op)));
          put_rec(ws, (lane < 12 && !(lane & 1)) ? WS_XO + (u32)(xg * 24 + j) * 128u + (u32)lane * 8u : OOB, op, on, T0, T1);
        }
        __builtin_amdgcn_s_sleep(DUTY1);
        {
          const int l6 = (lane * 43) >> 8;
          u32 off[1] = {WS_XO + (lane < 48 ? (u32)(xg * 24 + 8 * t + l6) * 128u + (u32)(lane - 6 * l6) * 16u : (u32)(xg * 24 + 8 * t) * 128u)}, d0[1], d1[1];
          get_recs<1, 16>(ws, off, T0, T1, d0, d1, dead);
          if (lane < 48) lp<u32x2>(L, L_OB)[lane] = u32x2{d0[0], d1[0]};
          asm volatile("s_waitcnt lgkmcnt(0)" ::: "memory");
        }
        if (t == 2) STAMP(5);
#pragma unroll 1
        for (int vw = 0; vw < 5; ++vw) {
          u32x4 wp[6];
          load_wo(L, vw, lane, wp);
          f32x2 y = gemv16(lp<u32>(L, L_OB), wp, lane);
          if (t > 0) y += lp<f32x2>(L, L_Y)[8 * vw + j8];
          put_rec(ws, lane < 8 ? WS_XC + (u32)(xg * 24 + j) * 640u + (u32)(8 * vw + lane) * 16u : OOB, __float_as_uint(y.x), __float_as_uint(y.y), T0, T1);
        }
      }
      if (tj == t + 1) {
        __builtin_amdgcn_s_sleep(127); __builtin_amdgcn_s_sleep(DUTY2);
        u32 off[1] = {WS_XC + (u32)(xg * 24 + j - 8) * 640u + (u32)(lane < 40 ? lane : 0) * 16u}, d0[1], d1[1];
        get_recs<1, 32>(ws, off, T0, T1, d0, d1, dead);
        if (lane < 40) lp<u32x2>(L, L_Y)[lane] = u32x2{d0[0], d1[0]};
      }
      if (t == 2) STAMP(13);
    }
    if ((CPF & 2) && tj < 2) { wait_flag<8>(FL + F_BLK, 1u, dead); code_touch(2 * j); code_touch(2 * j + 1); }
  } else {
    u32 xs[5]; u16 xr[3]; u32x4 wqv[3][3]; u32x4 ring[RING][2];
#pragma unroll
    for (int m = 0; m < 5; ++m) xs[m] = __builtin_amdgcn_raw_buffer_load_b32(rx, (lane + 64 * m) * 4, 0, 0);
    {
      const u32 row = (u32)(80 * (j & 7) + 20 * w + r);
      const u32 o = row * (u32)(H * DH * 2) + (u32)(8 * (j >> 3) + xg) * 384u + (u32)j8 * 16u;
#pragma unroll
      for (int g = 0; g < 3; ++g) {
        const u32 og = (g < 2 || r < 4) ? o : OOB, ox = (g < 2 || r < 4) ? row * 2u : OOB;
#pragma unroll
        for (int i = 0; i < 3; ++i) wqv[g][i] = __builtin_amdgcn_raw_buffer_load_b128(rq, og + i * 128, g * 8 * (H * DH * 2), AUX_NT);
        xr[g] = __builtin_amdgcn_raw_buffer_load_b16(rx, ox, g * 16, 0);
      }
    }
    ev = __builtin_amdgcn_raw_buffer_load_b64(ws, eoff, 0, AUX_SC1);
    xme = __builtin_amdgcn_raw_buffer_load_b32(rx, tid * 4, 0, 0);
    SB();
    auto issue = [&](int slot, int n, int i) {
      const rsrc_t rs = mk(n < 64 ? a.k : a.v, n < 128 ? HEADB * H : 0u);
      const u32 so = step_off(n & 63);
      ring[slot][0] = __builtin_amdgcn_raw_buffer_load_b128(rs, voff + i * 128, so, AUX_NT);
      ring[slot][1] = __builtin_amdgcn_raw_buffer_load_b128(rs, voff + 3072 + i * 128, so, AUX_NT);
      SB();
    };
#pragma unroll
    for (int u = 0; u < RING; ++u) issue(u, u / 3, u % 3);
    float acc[24];
    {
      float s = 0.f;
#pragma unroll
      for (int m = 0; m < 5; ++m) s += lo_f(xs[m]) + hi_f(xs[m]);
      const float mean = wave_sum(s) * (1.f / D);
      STAMP(18);
      float q2 = 0.f;
#pragma unroll
      for (int m = 0; m < 5; ++m) { const float d0 = lo_f(xs[m]) - mean, d1 = hi_f(xs[m]) - mean; q2 += d0 * d0 + d1 * d1; }
      const float var = rbf(wave_sum(q2) * (1.f / D));
      const float mb = rbf(mean), inv = __builtin_amdgcn_rcpf(rbf(__builtin_amdgcn_sqrtf(rbf(var + EPS))));
#pragma unroll
      for (int e = 0; e < 24; ++e) acc[e] = 0.f;
#pragma unroll
      for (int g = 0; g < 3; ++g) {
        const u32 xl = pk_bf16(rbf(lo_f(xr[g]) - mb) * inv, 0.f), xh = xl << 16;
#pragma unroll
        for (int i = 0; i < 3; ++i)
#pragma unroll
          for (int e = 0; e < 4; ++e) {
            acc[i * 8 + 2 * e] = dot2(wqv[g][i][e], xl, acc[i * 8 + 2 * e]);
            acc[i * 8 + 2 * e + 1] = dot2(wqv[g][i][e], xh, acc[i * 8 + 2 * e + 1]);
          }
      }
      flush24(acc, lp<float>(L, L_PART + w * 768), lane);
    }
    STAMP(1);
    bar();
    u32x4 wov[8];
    {
      const rsrc_t ro = mk(a.wo, H * DH * D * 2);
#pragma unroll
      for (int n = 0; n < 8; ++n) {
        const u32 c = (u32)(480 * w + 64 * n + lane), row = (c * 6554u) >> 16;
        const u32 oo = ((u32)(8 * tj + xg) * DH + row) * (D * 2) + 160u * c8 + (c - 10u * row) * 16u;
        wov[n] = __builtin_amdgcn_raw_buffer_load_b128(ro, (n < 7 || lane < 32) ? oo : OOB, 0, AUX_NT);
      }
    }
    SB();
    make_tag();
    bar();
    STAMP(2);
    u32 nxt = (u32)rt() * 16u;
#define PACE_STEP() do { u32 now_ = (u32)rt() * 16u; \
      while ((int)(nxt - now_) > 0) { __builtin_amdgcn_s_sleep(1); now_ = (u32)rt() * 16u; } \
      const u32 lo_ = now_ - PACE_BURST * 16u; if ((int)(lo_ - nxt) > 0) nxt = lo_; nxt += PACE; } while (0)
    LAS u32x4* sp = lp<u32x4>(L, L_S + w * 2048) + r;
    const float SCALE = 0.07216878364870322f;
    u32x4 q[3];
    auto k_block = [&](int bk, bool first) {
      u32x4 sc;
#pragma unroll
      for (int st = 0; st < 4; ++st) {
        PACE_STEP();
        float d[2] = {0.f, 0.f};
#pragma unroll
        for (int i = 0; i < 3; ++i) {
          const int u = 3 * st + i, slot = u % RING;
#pragma unroll
          for (int e = 0; e < 4; ++e) { d[0] = dot2(ring[slot][0][e], q[i][e], d[0]); d[1] = dot2(ring[slot][1][e], q[i][e], d[1]); }
          asm volatile("" : "+v"(d[0]), "+v"(d[1]));
          SB();
          issue(slot, 4 * bk + (u + RING) / 3, (u + RING) % 3);
        }
#pragma unroll
        for (int g = 0; g < 2; ++g) { d[g] += dpp<X1>(d[g]); d[g] += dpp<X2>(d[g]); d[g] += dpp<X7>(d[g]); }
        const u32 s1 = pk_bf16(d[0], d[1]);
        sc[st] = pk_bf16(lo_f(s1) * SCALE, hi_f(s1) * SCALE);
        if (first && st == 1) {
#pragma unroll
          for (int n = 0; n < 8; ++n) if (n < 7 || lane < 32) lp<u32x4>(L, L_WO)[480 * w + 64 * n + lane] = wov[n];
        }
      }
      if (j8 == 0) sp[bk * 8] = sc;
    };
    auto load_q = [&](int t) {
#pragma unroll
      for (int i = 0; i < 3; ++i) q[i] = lp<u32x4>(L, L_Q + t * 384)[j8 + 8 * i];
    };
    load_q(0);
    k_block(0, true);
#pragma unroll 1
    for (int bk = 1; bk < 16; ++bk) {
      if ((bk & 3) == 0) { signal_add((LAS u32*)FL + F_K + (bk >> 2) - 1, lane); wait_flag<1>(FL + F_Q, 1u, dead); load_q(bk >> 2); }
      k_block(bk, false);
    }
    signal_add((LAS u32*)FL + F_K + 3, lane);
    STAMP(3);
#pragma unroll 1
    for (int bk = 0; bk < 16; ++bk) {
      if ((bk & 3) == 0) {
        wait_flag<1>(FL + F_P + (bk >> 2), 1u, dead);
#pragma unroll
        for (int e = 0; e < 24; ++e) acc[e] = 0.f;
      }
      const u32x4 pp = sp[bk * 8];
      if (bk == CPB) FL[F_BLK] = 1u;
#pragma unroll
      for (int st = 0; st < 4; ++st) {
        if (3 * st + RING < 12 || bk < 15) PACE_STEP();
#pragma unroll
        for (int i = 0; i < 3; ++i) {
          const int u = 3 * st + i, slot = u % RING;
#pragma unroll
          for (int e = 0; e < 4; ++e) {
            acc[i * 8 + 2 * e] = dot2(zip_lo(ring[slot][0][e], ring[slot][1][e]), pp[st], acc[i * 8 + 2 * e]);
            acc[i * 8 + 2 * e + 1] = dot2(zip_hi(ring[slot][0][e], ring[slot][1][e]), pp[st], acc[i * 8 + 2 * e + 1]);
          }
#pragma unroll
          for (int e = 0; e < 8; ++e) asm volatile("" : "+v"(acc[i * 8 + e]));
          SB();
          issue(slot, 64 + 4 * bk + (u + RING) / 3, (u + RING) % 3);
        }
      }
      if ((bk & 3) == 3) {
        if (bk == 15) STAMP(4); else STAMP(14 + (bk >> 2));
        flush24(acc, lp<float>(L, L_PART + ((bk >> 2) * 4 + w) * 768), lane);
        if (bk < 15) signal_add((LAS u32*)FL + F_V + (bk >> 2), lane);
      }
    }
    {
      asm volatile("s_waitcnt lgkmcnt(0)" ::: "memory");
      u32 old = 0u;
      if (lane == 0) old = __hip_atomic_fetch_add((LAS u32*)FL + F_L, 1u, __ATOMIC_RELAXED, __HIP_MEMORY_SCOPE_WORKGROUP);
      if (__builtin_amdgcn_readfirstlane(old) == 3u) {
        asm volatile("" ::: "memory");
        publish192(ws, lp<f32x2>(L, L_PART + 3 * 3072), WS_X2 + (u32)(((3 * 8 + xg) * 32 + j) * 96) * 16u, lane, T0, T1);
        STAMP(6);
      }
      SB();
    }
    {
      const u32 og = r < 5 ? (u32)(20 * j + 10 * (w >> 1) + r) * (F * 2u) + 384u * xg + (u32)j8 * 16u : OOB;
      const u32 od = (lane & 15) < 12 ? (u32)(48 * j + 12 * w + (lane >> 4)) * (D * 2u) + ((160u * xg) & ~63u) + (u32)(lane & 15) * 16u : OOB;
      u32 v[9], keep = 0u;
#pragma unroll
      for (int n = 0; n < 6; ++n) v[n] = __builtin_amdgcn_raw_buffer_load_b32(mk((w & 1) ? a.wu : a.wg, D * F * 2), og + (n % 3) * 128, (n / 3) * 5 * (F * 2), 0);
#pragma unroll
      for (int n = 0; n < 3; ++n) v[6 + n] = __builtin_amdgcn_raw_buffer_load_b32(mk(a.wd, F * D * 2), od, n * 4 * (D * 2), 0);
      SB();
#pragma unroll
      for (int n = 0; n < 9; ++n) keep |= v[n];
      asm volatile("" :: "v"(keep));
    }
    __builtin_amdgcn_s_waitcnt(0x0F70);
    STAMP(5);
  }

  if (tj == 3) {
    u32x4 wp[6];
    load_wo(L, w, lane, wp);
#pragma unroll
    for (int e = 0; e < 6; ++e) asm volatile("" : "+v"(wp[e]));
    if (w != SVC) {
      __builtin_amdgcn_s_sleep(PRE);
      { u32 off[1] = {WS_X2 + (u32)(((3 * 8 + xg) * 32 + 8 * w + j8) * 96 + 95) * 16u}, d0[1], d1[1];
        get_recs<1, 2>(ws, off, T0, T1, d0, d1, dead); }
      gather_o<1, SLP_A>(ws, L, 3, xg, w, lane, T0, T1);
    }
    STAMP(7);
    bar();
    f32x2 y = lp<f32x2>(L, L_Y)[8 * w + j8];
    finish_o(L, lp<u32>(L, L_OB + w * 384), lane);
    y += gemv16(lp<u32>(L, L_OB + w * 384), wp, lane);
    put_rec(ws, WS_X3 + (u32)r * X3_REP + (u32)(xg * 320 + 40 * c8 + 8 * w + j8) * 16u, __float_as_uint(y.x), __float_as_uint(y.y), T0, T1);
    SB();
    STAMP(8);
    __builtin_amdgcn_s_barrier();
    SB();
    if (w < 3) {
      u32x4 da[8]; u32 db[8];
      const rsrc_t rd = mk(a.wd, F * D * 2);
      const u32 od = (u32)tid * (8u * D * 2u) + 160u * xg + 20u * c8;
#pragma unroll
      for (int n = 0; n < 8; ++n) {
        da[n] = __builtin_amdgcn_raw_buffer_load_b128(rd, od, n * D * 2, 0);
        db[n] = __builtin_amdgcn_raw_buffer_load_b32(rd, od + 16u, n * D * 2, 0);
      }
      u32 dp[4][10];
#pragma unroll
      for (int n = 0; n < 4; ++n) {
#pragma unroll
        for (int e = 0; e < 4; ++e) { dp[n][2 * e] = zip_lo(da[2 * n][e], da[2 * n + 1][e]); dp[n][2 * e + 1] = zip_hi(da[2 * n][e], da[2 * n + 1][e]); }
        dp[n][8] = zip_lo(db[2 * n], db[2 * n + 1]); dp[n][9] = zip_hi(db[2 * n], db[2 * n + 1]);
#pragma unroll
        for (int e = 0; e < 10; ++e) asm volatile("" : "+v"(dp[n][e]));
      }
      STAMP(10);
      u32 off[2] = {WS_X4 + (u32)xg * X4_REP + (u32)tid * 128u, WS_X4 + (u32)xg * X4_REP + (u32)tid * 128u + 16u}, d0[2], d1[2];
      get_recs<2, SLP_C>(ws, off, T0, T1, d0, d1, dead);
      STAMP(11);
      float c[10], c5[5];
#pragma unroll
      for (int e = 0; e < 10; ++e) c[e] = dot2(dp[3][e], d1[1], dot2(dp[2][e], d0[1], dot2(dp[1][e], d1[0], dot2(dp[0][e], d0[0], 0.f))));
#pragma unroll
      for (int e = 0; e < 5; ++e) c5[e] = tsum32(c[e], c[e + 5]);
      const float v0 = row_sum(tsum16(c5[0], c5[1])), v1 = row_sum(tsum16(c5[2], c5[3])), v2 = row_sum(tsum16(c5[4], c5[4]));
      if ((lane & 15) == 0) {
        LAS float* dn = lp<float>(L, L_DN) + w * 16 + 5 * (lane >> 5);
        const int b4 = (lane >> 4) & 1;
        dn[b4] = v0; dn[2 + b4] = v1; dn[4] = v2;
      }
    } else if (w == 3) {
      const int cp = lane & 7;
      u32 off[1] = {WS_X3 + (u32)xg * X3_REP + (u32)(r * 320 + 40 * xg + 5 * c8 + (cp < 5 ? cp : 0)) * 16u}, d0[1], d1[1];
      const u32 xv = __builtin_amdgcn_raw_buffer_load_b32(rx, (40 * xg + 5 * c8 + (cp < 5 ? cp : 0)) * 4, 0, 0);
      __builtin_amdgcn_s_sleep(PRE);
      get_recs<1, 8>(ws, off, T0, T1, d0, d1, dead);
      float s0 = __uint_as_float(d0[0]), s1 = __uint_as_float(d1[0]);
      s0 = tsum32(s0, s0); s1 = tsum32(s1, s1); s0 = tsum16(s0, s0); s1 = tsum16(s1, s1); s0 += dpp<R8>(s0); s1 += dpp<R8>(s1);
      const u32 at = pk_bf16(s0, s1);
      if (lane < 5) lp<u32>(L, L_H)[lane] = pk_bf16(lo_f(xv) + lo_f(at), hi_f(xv) + hi_f(at));
    }
    bar();
    if (tid < 10) {
      const float s = rbf((lp<float>(L, L_DN)[tid] + lp<float>(L, L_DN)[16 + tid]) + lp<float>(L, L_DN)[32 + tid]);
      a.out[80 * xg + 10 * c8 + tid] = (u16)pk_bf16(lo_f(lp<u16>(L, L_H)[tid]) + s, 0.f);
    }
  } else {
    bar();
    u32 gp[16];
    {
      const rsrc_t rg = mk(a.wg, D * F * 2), ru = mk(a.wu, D * F * 2);
      const u32 og = (u32)tid * (2u * F * 2u) + 384u * xg + 16u * j;
      const u32x4 A = __builtin_amdgcn_raw_buffer_load_b128(rg, og, 0, 0), B = __builtin_amdgcn_raw_buffer_load_b128(rg, og, F * 2, 0);
      const u32x4 C = __builtin_amdgcn_raw_buffer_load_b128(ru, og, 0, 0), E = __builtin_amdgcn_raw_buffer_load_b128(ru, og, F * 2, 0);
#pragma unroll
      for (int e = 0; e < 4; ++e) {
        gp[2 * e] = zip_lo(A[e], B[e]); gp[2 * e + 1] = zip_hi(A[e], B[e]);
        gp[8 + 2 * e] = zip_lo(C[e], E[e]); gp[8 + 2 * e + 1] = zip_hi(C[e], E[e]);
      }
#pragma unroll
      for (int e = 0; e < 16; ++e) asm volatile("" : "+v"(gp[e]));
    }
    STAMP(8);
    if (w == SVC) {
      u32 off[1] = {WS_X3 + (u32)xg * X3_REP + (u32)(r * 320 + 40 * j8 + 39) * 16u}, d0[1], d1[1];
      get_recs<1, 4>(ws, off, T0, T1, d0, d1, dead);
    }
    bar();
    u32 hp;
    {
      u32 off[8], d0[8], d1[8];
#pragma unroll
      for (int k = 0; k < 8; ++k) off[k] = WS_X3 + (u32)xg * X3_REP + (u32)(k * 320 + tid) * 16u;
      get_recs<8, SLP_B>(ws, off, T0, T1, d0, d1, dead);
      auto sum8 = [](const u32 (&d)[8]) {
        float t[4];
#pragma unroll
        for (int k = 0; k < 4; ++k) t[k] = __uint_as_float(d[k]) + __uint_as_float(d[k + 4]);
        return (t[0] + t[2]) + (t[1] + t[3]);
      };
      const u32 at = pk_bf16(sum8(d0), sum8(d1));
      hp = pk_bf16(lo_f(xme) + lo_f(at), hi_f(xme) + hi_f(at));
    }
    STAMP(9);
    const float h0 = lo_f(hp), h1 = hi_f(hp);
    {
      const float mw = wave_sum(h0 + h1) * (1.f / 128.f);
      const float m2 = wave_sum((h0 - mw) * (h0 - mw) + (h1 - mw) * (h1 - mw));
      if (lane == 0) lp<f32x2>(L, L_ST)[w] = f32x2{mw, m2};
    }
    bar();
    if (b == 0 && tid == 0) {
      const u64 e1 = (((u64)ev.y << 32) | ev.x) + 1ull;
      __builtin_amdgcn_raw_buffer_store_b64(u32x2{(u32)e1, (u32)(e1 >> 32)}, ws, WS_E, 0, AUX_SC1);
    }
    f32x2 st[5];
#pragma unroll
    for (int ww = 0; ww < 5; ++ww) st[ww] = lp<f32x2>(L, L_ST)[ww];
    const float mean = (((st[0].x + st[1].x) + (st[2].x + st[3].x)) + st[4].x) * 0.2f;
    float m2 = 0.f;
#pragma unroll
    for (int ww = 0; ww < 5; ++ww) m2 += st[ww].y + 128.f * (st[ww].x - mean) * (st[ww].x - mean);
    const float var = rbf(m2 * (1.f / D));
    const float mb = rbf(mean), inv = __builtin_amdgcn_rcpf(rbf(__builtin_amdgcn_sqrtf(rbf(var + EPS))));
    const u32 dd = pk_bf16(h0 - mb, h1 - mb);
    const u32 hn = pk_bf16(lo_f(dd) * inv, hi_f(dd) * inv);
    float g[16];
#pragma unroll
    for (int e = 0; e < 16; ++e) g[e] = dot2(gp[e], hn, 0.f);
    float g8[8], g4[4], g2[2];
#pragma unroll
    for (int e = 0; e < 8; ++e) g8[e] = tsum32(g[e], g[e + 8]);
#pragma unroll
    for (int e = 0; e < 4; ++e) g4[e] = tsum16(g8[e], g8[e + 4]);
    const bool b3 = (lane & 8) != 0, b2 = (lane & 4) != 0;
#pragma unroll
    for (int e = 0; e < 2; ++e) g2[e] = (b3 ? g4[e + 2] : g4[e]) + dpp<R8>(b3 ? g4[e] : g4[e + 2]);
    float g1 = (b2 ? g2[1] : g2[0]) + dpp<X7>(b2 ? g2[0] : g2[1]);
    g1 += dpp<X2>(g1); g1 += dpp<X1>(g1);
    if ((lane & 3) == 0) lp<float>(L, L_GU)[w * 16 + (lane >> 2)] = g1;
    bar();
    if (w == 0) {
      float gs = 0.f, us = 0.f;
#pragma unroll
      for (int ww = 0; ww < 5; ++ww) { gs += lp<float>(L, L_GU)[ww * 16 + j8]; us += lp<float>(L, L_GU)[ww * 16 + 8 + j8]; }
      const u32 gu = pk_bf16(gs, us);
      const float sg = rbf(__builtin_amdgcn_rcpf(1.f + __builtin_amdgcn_exp2f(-1.4426950408889634f * lo_f(gu))));
      const float f = sg * hi_f(gu);
      const u32 d0 = pk_bf16(dpp<0x00>(f), dpp<0x55>(f)), d1 = pk_bf16(dpp<0xAA>(f), dpp<0xFF>(f));
      put_rec(ws, (lane & 3) == 0 ? WS_X4 + (u32)r * X4_REP + (u32)(xg * 24 + j) * 128u + (u32)(lane & 4) * 4u : OOB, d0, d1, T0, T1);
    }
    STAMP(10);
  }
  STAMP(12);
  asm volatile(".Lfb_end:");
}
}

extern "C" void kernel_launch(void* const* d_in, const int* in_sizes, int n_in, void* d_out, int out_size, void* d_ws, size_t ws_size,
                              hipStream_t stream) {
  Args a;
  a.x = (const u16*)d_in[0]; a.k = (const u16*)d_in[1]; a.v = (const u16*)d_in[2]; a.wq = (const u16*)d_in[3]; a.wo = (const u16*)d_in[4];
  a.wg = (const u16*)d_in[6]; a.wu = (const u16*)d_in[7]; a.wd = (const u16*)d_in[8];
  a.out = (u16*)d_out; a.ws = (unsigned char*)d_ws;
  hipLaunchKernelGGL(fused_block, dim3(256), dim3(NTHR), 0, stream, a);
}
```

```cpp
#include <hip/hip_runtime.h>
#include <hip/hip_bf16.h>
#include <stdint.h>

#define STAMPS 0
#define PACE 1340
#define RING 6
#define PFT 1
#define BG 2
#define SENT_A 1
#define SENT_B 1
#define SLP_A 0
#define SLP_B 0
#define SLP_C 2
#define NOPF 0
#define DUTY0 40
#define DUTY1 10
#define DUTY2 32
#define PFWAIT 1
#define CPF 3
#define CPB 14
#define TCH 3
#define PADQ 0
#define PADV 0
#define PADE 0
#define QDELAY 32
#define PRE 10
#define PACE_BURST 5000

typedef unsigned u32;
typedef unsigned long long u64;
typedef unsigned short u16;
typedef u32 u32x2 __attribute__((ext_vector_type(2)));
typedef u32 u32x3 __attribute__((ext_vector_type(3)));
typedef u32 u32x4 __attribute__((ext_vector_type(4)));
typedef float f32x2 __attribute__((ext_vector_type(2)));
typedef float f32x4 __attribute__((ext_vector_type(4)));
typedef __bf16 bf16x2 __attribute__((ext_vector_type(2)));
typedef __amdgpu_buffer_rsrc_t rsrc_t;
#define LAS __attribute__((address_space(3)))

namespace {
constexpr int D = 640, H = 32, SK = 32768, DH = 192, F = 1536;
constexpr u32 HEADB = (u32)SK * DH * 2;
constexpr int NTHR = 320, SVC = 4;
constexpr u32 OOB = 0x80000000u;
constexpr int AUX_NT = 2, AUX_SC1 = 16;
constexpr u32 WS_E = 0, WS_X1 = 4096, WS_X2 = WS_X1 + 4 * 8 * 32 * 128, WS_X0 = WS_X2 + 4 * 8 * 32 * 1536, WS_XC = WS_X0 + 8 * 32 * 1536,
              WS_XO = WS_XC + 8 * 24 * 640, WS_X3 = WS_XO + 8 * 24 * 128, X3_REP = 8 * 320 * 16, WS_X4 = WS_X3 + 8 * X3_REP, X4_REP = 192 * 128, WS_BYTES = WS_X4 + 8 * X4_REP;
constexpr u32 WS_STAMP = 4u << 20;
static_assert(WS_BYTES <= WS_STAMP && WS_XC % 128 == 0 && WS_X3 % 128 == 0 && WS_X4 % 128 == 0, "ws");
constexpr int L_Q = 0, L_Y = 1536, L_FLAG = 2048, L_ST = 2176, L_GU = 2240, L_DN = 2560, L_H = 2752, L_OB = 3072, L_RED = 5120,
              L_S = 8192, L_PART = 16384, L_WO = 28672, L_WP = 59392, LDS_BYTES = L_WP + 30720;
constexpr int F_K = 0, F_P = 4, F_V = 8, F_O = 12, F_DEAD = 13, F_Q = 14, F_L = 15, F_BLK = 16;
constexpr u32 SPIN_G = 1u << 17, SPIN_L = 1u << 21;

struct Args {
  const u16 *x, *k, *v, *wq, *wo, *wg, *wu, *wd;
  u16* out;
  unsigned char* ws;
};

__device__ __forceinline__ float dot2(u32 a, u32 b, float c) {
  return __builtin_amdgcn_fdot2_f32_bf16(__builtin_bit_cast(bf16x2, a), __builtin_bit_cast(bf16x2, b), c, false);
}
__device__ __forceinline__ u32 pk_bf16(float lo, float hi) {
  bf16x2 r = {(__bf16)lo, (__bf16)hi};
  return __builtin_bit_cast(u32, r);
}
__device__ __forceinline__ float lo_f(u32 a) { return __uint_as_float(a << 16); }
__device__ __forceinline__ float hi_f(u32 a) { return __uint_as_float(a & 0xffff0000u); }
__device__ __forceinline__ float rbf(float a) { return lo_f(pk_bf16(a, a)); }
__device__ __forceinline__ u32 zip_lo(u32 a, u32 b) { return __builtin_amdgcn_perm(b, a, 0x05040100u); }
__device__ __forceinline__ u32 zip_hi(u32 a, u32 b) { return __builtin_amdgcn_perm(b, a, 0x07060302u); }
template <int CTRL> __device__ __forceinline__ float dpp(float x) {
  return __builtin_bit_cast(float, __builtin_amdgcn_mov_dpp(__builtin_bit_cast(int, x), CTRL, 0xf, 0xf, true));
}
constexpr int X1 = 0xB1, X2 = 0x4E, X7 = 0x141, R1 = 0x121, R2 = 0x122, R4 = 0x124, R8 = 0x128;
__device__ __forceinline__ float tsum32(float a, float b) {
  auto s = __builtin_amdgcn_permlane32_swap(__float_as_uint(a), __float_as_uint(b), false, false);
  return __uint_as_float(s[0]) + __uint_as_float(s[1]);
}
__device__ __forceinline__ float tsum16(float a, float b) {
  auto s = __builtin_amdgcn_permlane16_swap(__float_as_uint(a), __float_as_uint(b), false, false);
  return __uint_as_float(s[0]) + __uint_as_float(s[1]);
}
__device__ __forceinline__ float row_sum(float x) { x += dpp<R8>(x); x += dpp<X7>(x); x += dpp<X2>(x); x += dpp<X1>(x); return x; }
__device__ __forceinline__ float row_max(float x) {
  x = fmaxf(x, dpp<R8>(x)); x = fmaxf(x, dpp<X7>(x)); x = fmaxf(x, dpp<X2>(x)); x = fmaxf(x, dpp<X1>(x)); return x;
}
__device__ __forceinline__ float wave_sum(float x) { x = row_sum(x); x = tsum16(x, x); return tsum32(x, x); }
__device__ __forceinline__ float wave_max(float x) {
  x = row_max(x);
  auto s = __builtin_amdgcn_permlane16_swap(__float_as_uint(x), __float_as_uint(x), false, false);
  x = fmaxf(__uint_as_float(s[0]), __uint_as_float(s[1]));
  auto t = __builtin_amdgcn_permlane32_swap(__float_as_uint(x), __float_as_uint(x), false, false);
  return fmaxf(__uint_as_float(t[0]), __uint_as_float(t[1]));
}
__device__ __forceinline__ rsrc_t mk(const void* p, u32 bytes) { return __builtin_amdgcn_make_buffer_rsrc((void*)p, 0, bytes, 0x00020000); }
__device__ __forceinline__ void bar() {
  asm volatile("s_waitcnt lgkmcnt(0)" ::: "memory");
  __builtin_amdgcn_s_barrier();
  asm volatile("" ::: "memory");
}
__device__ __forceinline__ void touch2(const void* p, const void* q) {
  u32 d0, d1;
  asm volatile("s_load_dword %0, %2, 0x0\n\ts_load_dword %1, %3, 0x0\n\ts_waitcnt lgkmcnt(0)" : "=&s"(d0), "=&s"(d1) : "s"(p), "s"(q) : "memory");
}
__device__ __forceinline__ u64 rt() { return __builtin_amdgcn_s_memrealtime(); }
#define SB() __builtin_amdgcn_sched_barrier(0)

template <typename T> __device__ __forceinline__ LAS T* lp(LAS unsigned char* l, int off) { return (LAS T*)(l + off); }

__device__ __forceinline__ void put_rec(rsrc_t ws, u32 off, u32 d0, u32 d1, u32 T0, u32 T1) {
  __builtin_amdgcn_raw_buffer_store_b128(u32x4{d0, d1, d0 ^ T0, d1 ^ T1}, ws, off, 0, AUX_SC1);
}
template <int N, int SLEEP>
__device__ __forceinline__ void get_recs(rsrc_t ws, const u32 (&off)[N], u32 T0, u32 T1, u32 (&d0)[N], u32 (&d1)[N], volatile LAS u32* dead) {
#pragma unroll 1
  for (u32 spin = 0;; ++spin) {
    asm volatile("" ::: "memory");
    u32x4 t[N];
#pragma unroll
    for (int k = 0; k < N; ++k) t[k] = __builtin_amdgcn_raw_buffer_load_b128(ws, off[k], 0, AUX_SC1);
    SB();
    u32 bad = 0u;
#pragma unroll
    for (int k = 0; k < N; ++k) { bad |= (t[k].x ^ t[k].z ^ T0) | (t[k].y ^ t[k].w ^ T1); d0[k] = t[k].x; d1[k] = t[k].y; }
    if (!__builtin_amdgcn_ballot_w64(bad != 0u)) break;
    if (SLEEP) __builtin_amdgcn_s_sleep(SLEEP);
    if ((spin & 255u) == 255u) { if (*dead) break; if (spin > SPIN_G) { *dead = 1u; break; } }
  }
}
template <int SLEEP>
__device__ __forceinline__ void wait_flag(volatile LAS u32* f, u32 want, volatile LAS u32* dead) {
  for (u32 spin = 0; (u32)__builtin_amdgcn_readfirstlane(*f) < want; ++spin) {
    if (SLEEP) __builtin_amdgcn_s_sleep(SLEEP);
    if ((spin & 255u) == 255u) { if (*dead) break; if (spin > SPIN_L) { *dead = 1u; break; } }
  }
  asm volatile("" ::: "memory");
}
__device__ __forceinline__ void signal_add(LAS u32* f, int lane) {
  asm volatile("s_waitcnt lgkmcnt(0)" ::: "memory");
  if (lane == 0) __hip_atomic_fetch_add(f, 1u, __ATOMIC_RELAXED, __HIP_MEMORY_SCOPE_WORKGROUP);
}

#define STAMP(i) do { } while (0)

template <int G, int SLEEP>
__device__ __forceinline__ void gather8(rsrc_t ws, LAS unsigned char* L, u32 first, int lane, u32 T0, u32 T1, float (&s)[G][3]) {
  u32 off[12 * G], d0[12 * G], d1[12 * G];
  const u32 base = first + (u32)((lane >> 5) * 96 + (lane & 31)) * 16u;
#pragma unroll
  for (int n = 0; n < 4 * G; ++n)
#pragma unroll
    for (int pr = 0; pr < 3; ++pr) off[n * 3 + pr] = base + (u32)(2 * n * 96 + 32 * pr) * 16u;
  get_recs<12 * G, SLEEP>(ws, off, T0, T1, d0, d1, lp<u32>(L, L_FLAG) + F_DEAD);
#pragma unroll
  for (int g = 0; g < G; ++g)
#pragma unroll
    for (int pr = 0; pr < 3; ++pr) {
      const int k = 12 * g + pr;
      float a = (__uint_as_float(d0[k]) + __uint_as_float(d0[k + 3])) + (__uint_as_float(d0[k + 6]) + __uint_as_float(d0[k + 9]));
      float b = (__uint_as_float(d1[k]) + __uint_as_float(d1[k + 3])) + (__uint_as_float(d1[k + 6]) + __uint_as_float(d1[k + 9]));
      s[g][pr] = tsum32(a, b);
    }
}
template <int G, int SLEEP>
__device__ __forceinline__ void gather_o(rsrc_t ws, LAS unsigned char* L, int t, int xg, int vw, int lane, u32 T0, u32 T1) {
  float s[G][3];
  gather8<G, SLEEP>(ws, L, WS_X2 + (u32)(((t * 8 + xg) * 32 + 8 * vw) * 96) * 16u, lane, T0, T1, s);
#pragma unroll
  for (int g = 0; g < G; ++g)
#pragma unroll
    for (int pr = 0; pr < 3; ++pr) lp<float>(L, L_RED)[(((vw + g) * 3 + pr) * 32 + (lane & 31)) * 2 + (lane >> 5)] = s[g][pr];
}
__device__ __forceinline__ void flush24(const float (&acc)[24], LAS float* dst, int lane) {
#pragma unroll
  for (int i = 0; i < 3; ++i) {
    float s1[4], s2[2];
#pragma unroll
    for (int e = 0; e < 4; ++e) s1[e] = tsum32(acc[i * 8 + e], acc[i * 8 + e + 4]);
#pragma unroll
    for (int e = 0; e < 2; ++e) s2[e] = tsum16(s1[e], s1[e + 2]);
    const bool b3 = (lane & 8) != 0;
    const float keep = b3 ? s2[1] : s2[0], send = b3 ? s2[0] : s2[1];
    dst[64 * i + 8 * (lane & 7) + (lane >> 3)] = keep + dpp<R8>(send);
  }
}
__device__ __forceinline__ void publish192(rsrc_t ws, const LAS f32x2* src, u32 first, int lane, u32 T0, u32 T1) {
  const LAS f32x2* p0 = src + lane; const LAS f32x2* p1 = src + 64 + (lane & 31);
  const f32x2 s0 = (p0[0] + p0[96]) + (p0[192] + p0[288]), s1 = (p1[0] + p1[96]) + (p1[192] + p1[288]);
  put_rec(ws, first + (u32)lane * 16u, __float_as_uint(s0.x), __float_as_uint(s0.y), T0, T1);
  put_rec(ws, lane < 32 ? first + 1024u + (u32)lane * 16u : OOB, __float_as_uint(s1.x), __float_as_uint(s1.y), T0, T1);
}
__device__ __forceinline__ void pack_wo(LAS unsigned char* L, int vw, int lane) {
  const LAS u32* src = lp<u32>(L, L_WO) + (lane >> 3) * 960 + 8 * vw + (lane & 7);
#pragma unroll
  for (int m = 0; m < 12; m += 2) {
    const u32 A = src[80 * m], B = src[80 * m + 40], C = src[80 * m + 80], E = src[80 * m + 120];
    lp<u32x4>(L, L_WP)[(vw * 6 + m / 2) * 64 + lane] = u32x4{zip_lo(A, B), zip_hi(A, B), zip_lo(C, E), zip_hi(C, E)};
  }
}
__device__ __forceinline__ void load_wo(LAS unsigned char* L, int vw, int lane, u32x4 (&wp)[6]) {
#pragma unroll
  for (int m = 0; m < 6; ++m) wp[m] = lp<u32x4>(L, L_WP)[(vw * 6 + m) * 64 + lane];
}
__device__ __forceinline__ void finish_o(LAS unsigned char* L, LAS u32* ob, int lane) {
  const LAS f32x4* rp = lp<f32x4>(L, L_RED) + (lane < 48 ? lane : 0);
  const f32x4 s = (rp[0] + rp[48]) + (rp[96] + rp[144]);
  if (lane < 48) ((LAS u32x2*)ob)[lane] = u32x2{pk_bf16(s.x, s.y), pk_bf16(s.z, s.w)};
  asm volatile("s_waitcnt lgkmcnt(0)" ::: "memory");
}
__device__ __forceinline__ f32x2 gemv16(const LAS u32* ob, const u32x4 (&wp)[6], int lane) {
  const LAS u32x4* op = (const LAS u32x4*)ob + (lane >> 3) * 3;
  const u32x4 o0 = op[0], o1 = op[1], o2 = op[2];
  float y[4] = {0.f, 0.f, 0.f, 0.f};
#pragma unroll
  for (int m = 0; m < 12; ++m) {
    const u32 o = m < 4 ? o0[m & 3] : m < 8 ? o1[m & 3] : o2[m & 3];
    y[2 * (m & 1)] = dot2(wp[m >> 1][2 * (m & 1)], o, y[2 * (m & 1)]); y[2 * (m & 1) + 1] = dot2(wp[m >> 1][2 * (m & 1) + 1], o, y[2 * (m & 1) + 1]);
  }
  float y0 = y[0] + y[2], y1 = y[1] + y[3];
  y0 += dpp<R8>(y0); y1 += dpp<R8>(y1);
  y0 = tsum16(y0, y0); y1 = tsum16(y1, y1);
  return f32x2{tsum32(y0, y0), tsum32(y1, y1)};
}

__global__ void __launch_bounds__(NTHR, 1) fused_block(Args a) {
  __shared__ __attribute__((aligned(16))) unsigned char lds_raw[LDS_BYTES];
  LAS unsigned char* L = (LAS unsigned char*)lds_raw;
  const int tid = threadIdx.x, lane = tid & 63, w = __builtin_amdgcn_readfirstlane(tid >> 6);
  const int b = blockIdx.x, xg = b & 7, j = b >> 3;
  const int r = lane >> 3, j8 = lane & 7;
  const int tj = j >> 3, c8 = j & 7;
  volatile LAS u32* FL = lp<u32>(L, L_FLAG);
  volatile LAS u32* dead = FL + F_DEAD;
  STAMP(0);
  const u32 tstart = (u32)rt();
  auto pad = [&](u32 ticks) { while ((u32)rt() - tstart < ticks) __builtin_amdgcn_s_sleep(1); };
  const rsrc_t ws = mk(a.ws, WS_BYTES);
  const rsrc_t rx = mk(a.x, D * 2), rq = mk(a.wq, D * H * DH * 2);

  const float EPS = 1e-5f;
  u64 pc0; u32 clen;
  asm volatile("s_getpc_b64 %0\n.Lfb_begin:\n\ts_mov_b32 %1, .Lfb_end-.Lfb_begin" : "=s"(pc0), "=s"(clen));
  auto code_touch = [&](int piece) {
    const u32 v = __builtin_amdgcn_raw_buffer_load_b32(mk((const void*)pc0, clen), (u32)piece * 1024u + (u32)lane * 16u, 0, 0);
    asm volatile("" :: "v"(v));
  };
  u32 eoff = WS_E; asm volatile("" : "+v"(eoff));
  const u32 voff = (u32)r * 384u + (u32)j8 * 16u;
  const u32 sbase = (u32)xg * HEADB + (u32)j * 24576u + (u32)w * 6144u;
  auto step_off = [&](int n) -> u32 { return sbase + (u32)(n >> 4) * (8u * HEADB) + (u32)(n & 15) * (32u * 24576u); };
  u32x2 ev; u32 xme, T0, T1;
  auto make_tag = [&]() {
    u64 z = (((u64)ev.y << 32) | ev.x) + 0x9E3779B97F4A7C15ull;
    z = (z ^ (z >> 30)) * 0xBF58476D1CE4E5B9ull; z = (z ^ (z >> 27)) * 0x94D049BB133111EBull; z ^= z >> 31;
    T0 = __builtin_amdgcn_readfirstlane((u32)z) | 1u; T1 = __builtin_amdgcn_readfirstlane((u32)(z >> 32)) | 1u;
  };

  if (w == SVC) {
    ev = __builtin_amdgcn_raw_buffer_load_b64(ws, eoff, 0, AUX_SC1);
    xme = __builtin_amdgcn_raw_buffer_load_b32(rx, tid * 4, 0, 0);
    if (lane < 32) FL[lane] = 0u;
    if (CPF & 1) code_touch(j);
    if ((TCH & 1) && j >= 26) { const char* p = (const char*)a.k + (size_t)xg * HEADB + (size_t)(j - 25) * 2097152 - 64; touch2(p, p); }
    make_tag();
    STAMP(1);
    bar();
    publish192(ws, lp<f32x2>(L, L_PART), WS_X0 + (u32)((xg * 32 + j) * 96) * 16u, lane, T0, T1);
    __builtin_amdgcn_s_sleep(QDELAY);
#pragma unroll 1
    for (int t = 0; t < 4; ++t) {
      float s[1][3];
      gather8<1, 4>(ws, L, WS_X0 + (u32)((xg * 32 + 8 * t) * 96) * 16u, lane, T0, T1, s);
#pragma unroll
      for (int pr = 0; pr < 3; ++pr) lp<u16>(L, L_Q)[t * 192 + (32 * pr + (lane & 31)) * 2 + (lane >> 5)] = (u16)pk_bf16(s[0][pr], 0.f);
      if (t == 0) {
        STAMP(2); bar();
        if (TCH & 2) {
          auto at = [&](int n) -> const char* {
            if (n < 49) { const int sl = n / 7 + 1, pt = n % 7;
              return (const char*)(sl < 4 ? a.k : a.v) + (size_t)(8 * (sl & 3) + xg) * HEADB + (pt ? (size_t)pt * 2097152 - 64 : 0); }
            const int m = n - 49, e = (m & 1) * (D * F * 2 - 64);
            return m < 2 ? (const char*)a.wg + e : m < 4 ? (const char*)a.wu + e : m < 6 ? (const char*)a.wd + e : m < 8 ? (const char*)a.out : (const char*)a.ws + (m & 1) * (WS_BYTES - 64);
          };
          touch2(at(j), at(j + 32));
        }
      }
      __builtin_amdgcn_s_sleep(64);
    }
    signal_add((LAS u32*)FL + F_Q, lane);
#pragma unroll 1
    for (int t = 0; t < 4; ++t) {
      wait_flag<8>(FL + F_K + t, 4u, dead);
      if (t == 0) {
#pragma unroll 1
        for (int vw = 0; vw < 5; ++vw) pack_wo(L, vw, lane);
      }
      LAS u32x4* sp = lp<u32x4>(L, L_S + ((lane >> 4) * 4 + t) * 512 + (lane & 15) * 32);
      const u32x4 s0 = sp[0], s1 = sp[1];
      float s[16];
#pragma unroll
      for (int e = 0; e < 4; ++e) { s[2 * e] = lo_f(s0[e]); s[2 * e + 1] = hi_f(s0[e]); s[8 + 2 * e] = lo_f(s1[e]); s[8 + 2 * e + 1] = hi_f(s1[e]); }
      float m = s[0];
#pragma unroll
      for (int e = 1; e < 16; ++e) m = fmaxf(m, s[e]);
      m = wave_max(m);
      const float L2E = 1.4426950408889634f;
      float l = 0.f;
#pragma unroll
      for (int e = 0; e < 16; ++e) l += __builtin_amdgcn_exp2f((s[e] - m) * L2E);
      l = wave_sum(l);
      put_rec(ws, lane == 0 ? WS_X1 + (u32)((t * 8 + xg) * 32 + j) * 128u : OOB, __float_as_uint(m), __float_as_uint(l), T0, T1);
      u32 off[1] = {WS_X1 + (u32)((t * 8 + xg) * 32 + (lane & 31)) * 128u}, d0[1], d1[1];
      __builtin_amdgcn_s_sleep(32);
      get_recs<1, 32>(ws, off, T0, T1, d0, d1, dead);
      const float mj = __uint_as_float(d0[0]), lj = __uint_as_float(d1[0]);
      const float M = wave_max(mj);
      float Ls = row_sum(lj * __builtin_amdgcn_exp2f((mj - M) * L2E));
      Ls = tsum16(Ls, Ls);
      Ls = __uint_as_float(__builtin_amdgcn_readfirstlane(__float_as_uint(Ls)));
      const float inv = 1.f / Ls;
      u32x4 p0, p1;
#pragma unroll
      for (int e = 0; e < 4; ++e) {
        p0[e] = pk_bf16(__builtin_amdgcn_exp2f((s[2 * e] - M) * L2E) * inv, __builtin_amdgcn_exp2f((s[2 * e + 1] - M) * L2E) * inv);
        p1[e] = pk_bf16(__builtin_amdgcn_exp2f((s[8 + 2 * e] - M) * L2E) * inv, __builtin_amdgcn_exp2f((s[8 + 2 * e + 1] - M) * L2E) * inv);
      }
      sp[0] = p0; sp[1] = p1;
      signal_add((LAS u32*)FL + F_P + t, lane);
    }
#pragma unroll 1
    for (int t = 0; t < 3; ++t) {
      wait_flag<8>(FL + F_V + t, 4u, dead);
      if (t == 2) STAMP(3);
      publish192(ws, lp<f32x2>(L, L_PART + t * 3072), WS_X2 + (u32)(((t * 8 + xg) * 32 + j) * 96) * 16u, lane, T0, T1);
      if (tj == t) {
        __builtin_amdgcn_s_sleep(DUTY0);
        {
          const int rc = lane & 15;
          u32 off[8], d0[8], d1[8];
#pragma unroll
          for (int n = 0; n < 8; ++n) off[n] = WS_X2 + (u32)(((t * 8 + xg) * 32 + 4 * n + (lane >> 4)) * 96 + 12 * c8 + (rc < 12 ? rc : 0)) * 16u;
          get_recs<8, 32>(ws, off, T0, T1, d0, d1, dead);
          if (t == 2) STAMP(4);
          float s0 = 0.f, s1 = 0.f;
#pragma unroll
          for (int n = 0; n < 8; ++n) { s0 += __uint_as_float(d0[n]); s1 += __uint_as_float(d1[n]); }
          s0 = tsum16(s0, s0); s1 = tsum16(s1, s1); s0 = tsum32(s0, s0); s1 = tsum32(s1, s1);
          const u32 op = pk_bf16(s0, s1), on = __float_as_uint(dpp<X1>(__uint_as_float(op)));
          put_rec(ws, (lane < 12 && !(lane & 1)) ? WS_XO + (u32)(xg * 24 + j) * 128u + (u32)lane * 8u : OOB, op, on, T0, T1);
        }
        __builtin_amdgcn_s_sleep(DUTY1);
        {
          const int l6 = (lane * 43) >> 8;
          u32 off[1] = {WS_XO + (lane < 48 ? (u32)(xg * 24 + 8 * t + l6) * 128u + (u32)(lane - 6 * l6) * 16u : (u32)(xg * 24 + 8 * t) * 128u)}, d0[1], d1[1];
          get_recs<1, 16>(ws, off, T0, T1, d0, d1, dead);
          if (lane < 48) lp<u32x2>(L, L_OB)[lane] = u32x2{d0[0], d1[0]};
          asm volatile("s_waitcnt lgkmcnt(0)" ::: "memory");
        }
        if (t == 2) STAMP(5);
#pragma unroll 1
        for (int vw = 0; vw < 5; ++vw) {
          u32x4 wp[6];
          load_wo(L, vw, lane, wp);
          f32x2 y = gemv16(lp<u32>(L, L_OB), wp, lane);
          if (t > 0) y += lp<f32x2>(L, L_Y)[8 * vw + j8];
          put_rec(ws, lane < 8 ? WS_XC + (u32)(xg * 24 + j) * 640u + (u32)(8 * vw + lane) * 16u : OOB, __float_as_uint(y.x), __float_as_uint(y.y), T0, T1);
        }
      }
      if (tj == t + 1) {
        __builtin_amdgcn_s_sleep(127); __builtin_amdgcn_s_sleep(DUTY2);
        u32 off[1] = {WS_XC + (u32)(xg * 24 + j - 8) * 640u + (u32)(lane < 40 ? lane : 0) * 16u}, d0[1], d1[1];
        get_recs<1, 32>(ws, off, T0, T1, d0, d1, dead);
        if (lane < 40) lp<u32x2>(L, L_Y)[lane] = u32x2{d0[0], d1[0]};
      }
      if (t == 2) STAMP(13);
    }
    if ((CPF & 2) && tj < 2) { wait_flag<8>(FL + F_BLK, 1u, dead); code_touch(2 * j); code_touch(2 * j + 1); }
  } else {
    u32 xs[5]; u16 xr[3]; u32x4 wqv[3][3]; u32x4 ring[RING][2];
#pragma unroll
    for (int m = 0; m < 5; ++m) xs[m] = __builtin_amdgcn_raw_buffer_load_b32(rx, (lane + 64 * m) * 4, 0, 0);
    {
      const u32 row = (u32)(80 * (j & 7) + 20 * w + r);
      const u32 o = row * (u32)(H * DH * 2) + (u32)(8 * (j >> 3) + xg) * 384u + (u32)j8 * 16u;
#pragma unroll
      for (int g = 0; g < 3; ++g) {
        const u32 og = (g < 2 || r < 4) ? o : OOB, ox = (g < 2 || r < 4) ? row * 2u : OOB;
#pragma unroll
        for (int i = 0; i < 3; ++i) wqv[g][i] = __builtin_amdgcn_raw_buffer_load_b128(rq, og + i * 128, g * 8 * (H * DH * 2), AUX_NT);
        xr[g] = __builtin_amdgcn_raw_buffer_load_b16(rx, ox, g * 16, 0);
      }
    }
    ev = __builtin_amdgcn_raw_buffer_load_b64(ws, eoff, 0, AUX_SC1);
    xme = __builtin_amdgcn_raw_buffer_load_b32(rx, tid * 4, 0, 0);
    SB();
    auto issue = [&](int slot, int n, int i) {
      const rsrc_t rs = mk(n < 64 ? a.k : a.v, n < 128 ? HEADB * H : 0u);
      const u32 so = step_off(n & 63);
      ring[slot][0] = __builtin_amdgcn_raw_buffer_load_b128(rs, voff + i * 128, so, AUX_NT);
      ring[slot][1] = __builtin_amdgcn_raw_buffer_load_b128(rs, voff + 3072 + i * 128, so, AUX_NT);
      SB();
    };
#pragma unroll
    for (int u = 0; u < RING; ++u) issue(u, u / 3, u % 3);
    float acc[24];
    {
      float s = 0.f;
#pragma unroll
      for (int m = 0; m < 5; ++m) s += lo_f(xs[m]) + hi_f(xs[m]);
      const float mean = wave_sum(s) * (1.f / D);
      STAMP(18);
      float q2 = 0.f;
#pragma unroll
      for (int m = 0; m < 5; ++m) { const float d0 = lo_f(xs[m]) - mean, d1 = hi_f(xs[m]) - mean; q2 += d0 * d0 + d1 * d1; }
      const float var = rbf(wave_sum(q2) * (1.f / D));
      const float mb = rbf(mean), inv = __builtin_amdgcn_rcpf(rbf(__builtin_amdgcn_sqrtf(rbf(var + EPS))));
#pragma unroll
      for (int e = 0; e < 24; ++e) acc[e] = 0.f;
#pragma unroll
      for (int g = 0; g < 3; ++g) {
        const u32 xl = pk_bf16(rbf(lo_f(xr[g]) - mb) * inv, 0.f), xh = xl << 16;
#pragma unroll
        for (int i = 0; i < 3; ++i)
#pragma unroll
          for (int e = 0; e < 4; ++e) {
            acc[i * 8 + 2 * e] = dot2(wqv[g][i][e], xl, acc[i * 8 + 2 * e]);
            acc[i * 8 + 2 * e + 1] = dot2(wqv[g][i][e], xh, acc[i * 8 + 2 * e + 1]);
          }
      }
      flush24(acc, lp<float>(L, L_PART + w * 768), lane);
    }
    STAMP(1);
    bar();
    u32x4 wov[8];
    {
      const rsrc_t ro = mk(a.wo, H * DH * D * 2);
#pragma unroll
      for (int n = 0; n < 8; ++n) {
        const u32 c = (u32)(480 * w + 64 * n + lane), row = (c * 6554u) >> 16;
        const u32 oo = ((u32)(8 * tj + xg) * DH + row) * (D * 2) + 160u * c8 + (c - 10u * row) * 16u;
        wov[n] = __builtin_amdgcn_raw_buffer_load_b128(ro, (n < 7 || lane < 32) ? oo : OOB, 0, AUX_NT);
      }
    }
    SB();
    make_tag();
    bar();
    if (PADQ) pad(PADQ);
    STAMP(2);
    u32 nxt = (u32)rt() * 16u;
#define PACE_STEP() do { u32 now_ = (u32)rt() * 16u; \
      while ((int)(nxt - now_) > 0) { __builtin_amdgcn_s_sleep(1); now_ = (u32)rt() * 16u; } \
      const u32 lo_ = now_ - PACE_BURST * 16u; if ((int)(lo_ - nxt) > 0) nxt = lo_; nxt += PACE; } while (0)
    LAS u32x4* sp = lp<u32x4>(L, L_S + w * 2048) + r;
    const float SCALE = 0.07216878364870322f;
    u32x4 q[3];
    auto k_block = [&](int bk, bool first) {
      u32x4 sc;
#pragma unroll
      for (int st = 0; st < 4; ++st) {
        PACE_STEP();
        float d[2] = {0.f, 0.f};
#pragma unroll
        for (int i = 0; i < 3; ++i) {
          const int u = 3 * st + i, slot = u % RING;
#pragma unroll
          for (int e = 0; e < 4; ++e) { d[0] = dot2(ring[slot][0][e], q[i][e], d[0]); d[1] = dot2(ring[slot][1][e], q[i][e], d[1]); }
          asm volatile("" : "+v"(d[0]), "+v"(d[1]));
          SB();
          issue(slot, 4 * bk + (u + RING) / 3, (u + RING) % 3);
        }
#pragma unroll
        for (int g = 0; g < 2; ++g) { d[g] += dpp<X1>(d[g]); d[g] += dpp<X2>(d[g]); d[g] += dpp<X7>(d[g]); }
        const u32 s1 = pk_bf16(d[0], d[1]);
        sc[st] = pk_bf16(lo_f(s1) * SCALE, hi_f(s1) * SCALE);
        if (first && st == 1) {
#pragma unroll
          for (int n = 0; n < 8; ++n) if (n < 7 || lane < 32) lp<u32x4>(L, L_WO)[480 * w + 64 * n + lane] = wov[n];
        }
      }
      if (j8 == 0) sp[bk * 8] = sc;
    };
    auto load_q = [&](int t) {
#pragma unroll
      for (int i = 0; i < 3; ++i) q[i] = lp<u32x4>(L, L_Q + t * 384)[j8 + 8 * i];
    };
    load_q(0);
    k_block(0, true);
#pragma unroll 1
    for (int bk = 1; bk < 16; ++bk) {
      if ((bk & 3) == 0) { signal_add((LAS u32*)FL + F_K + (bk >> 2) - 1, lane); wait_flag<1>(FL + F_Q, 1u, dead); load_q(bk >> 2); }
      k_block(bk, false);
    }
    signal_add((LAS u32*)FL + F_K + 3, lane);
    STAMP(3);
#pragma unroll 1
    for (int bk = 0; bk < 16; ++bk) {
      if ((bk & 3) == 0) {
        wait_flag<1>(FL + F_P + (bk >> 2), 1u, dead);
#pragma unroll
        for (int e = 0; e < 24; ++e) acc[e] = 0.f;
      }
      const u32x4 pp = sp[bk * 8];
      if (bk == CPB) FL[F_BLK] = 1u;
#pragma unroll
      for (int st = 0; st < 4; ++st) {
        if (3 * st + RING < 12 || bk < 15) PACE_STEP();
#pragma unroll
        for (int i = 0; i < 3; ++i) {
          const int u = 3 * st + i, slot = u % RING;
#pragma unroll
          for (int e = 0; e < 4; ++e) {
            acc[i * 8 + 2 * e] = dot2(zip_lo(ring[slot][0][e], ring[slot][1][e]), pp[st], acc[i * 8 + 2 * e]);
            acc[i * 8 + 2 * e + 1] = dot2(zip_hi(ring[slot][0][e], ring[slot][1][e]), pp[st], acc[i * 8 + 2 * e + 1]);
          }
#pragma unroll
          for (int e = 0; e < 8; ++e) asm volatile("" : "+v"(acc[i * 8 + e]));
          SB();
          issue(slot, 64 + 4 * bk + (u + RING) / 3, (u + RING) % 3);
        }
      }
      if ((bk & 3) == 3) {
        if (PADV && bk == 15) pad(PADV);
        if (bk == 15) STAMP(4); else STAMP(14 + (bk >> 2));
        flush24(acc, lp<float>(L, L_PART + ((bk >> 2) * 4 + w) * 768), lane);
        if (bk < 15) signal_add((LAS u32*)FL + F_V + (bk >> 2), lane);
      }
    }
    {
      asm volatile("s_waitcnt lgkmcnt(0)" ::: "memory");
      u32 old = 0u;
      if (lane == 0) old = __hip_atomic_fetch_add((LAS u32*)FL + F_L, 1u, __ATOMIC_RELAXED, __HIP_MEMORY_SCOPE_WORKGROUP);
      if (__builtin_amdgcn_readfirstlane(old) == 3u) {
        asm volatile("" ::: "memory");
        publish192(ws, lp<f32x2>(L, L_PART + 3 * 3072), WS_X2 + (u32)(((3 * 8 + xg) * 32 + j) * 96) * 16u, lane, T0, T1);
        STAMP(6);
      }
      SB();
    }
    {
      const u32 og = r < 5 ? (u32)(20 * j + 10 * (w >> 1) + r) * (F * 2u) + 384u * xg + (u32)j8 * 16u : OOB;
      const u32 od = (lane & 15) < 12 ? (u32)(48 * j + 12 * w + (lane >> 4)) * (D * 2u) + ((160u * xg) & ~63u) + (u32)(lane & 15) * 16u : OOB;
      u32 v[9], keep = 0u;
#pragma unroll
      for (int n = 0; n < 6; ++n) v[n] = __builtin_amdgcn_raw_buffer_load_b32(mk((w & 1) ? a.wu : a.wg, D * F * 2), og + (n % 3) * 128, (n / 3) * 5 * (F * 2), 0);
#pragma unroll
      for (int n = 0; n < 3; ++n) v[6 + n] = __builtin_amdgcn_raw_buffer_load_b32(mk(a.wd, F * D * 2), od, n * 4 * (D * 2), 0);
      SB();
#pragma unroll
      for (int n = 0; n < 9; ++n) keep |= v[n];
      asm volatile("" :: "v"(keep));
    }
    __builtin_amdgcn_s_waitcnt(0x0F70);
    STAMP(5);
  }

  if (tj == 3) {
    u32x4 wp[6];
    load_wo(L, w, lane, wp);
#pragma unroll
    for (int e = 0; e < 6; ++e) asm volatile("" : "+v"(wp[e]));
    if (w != SVC) {
      __builtin_amdgcn_s_sleep(PRE);
      { u32 off[1] = {WS_X2 + (u32)(((3 * 8 + xg) * 32 + 8 * w + j8) * 96 + 95) * 16u}, d0[1], d1[1];
        get_recs<1, 2>(ws, off, T0, T1, d0, d1, dead); }
      gather_o<1, SLP_A>(ws, L, 3, xg, w, lane, T0, T1);
    }
    STAMP(7);
    bar();
    f32x2 y = lp<f32x2>(L, L_Y)[8 * w + j8];
    finish_o(L, lp<u32>(L, L_OB + w * 384), lane);
    y += gemv16(lp<u32>(L, L_OB + w * 384), wp, lane);
    put_rec(ws, WS_X3 + (u32)r * X3_REP + (u32)(xg * 320 + 40 * c8 + 8 * w + j8) * 16u, __float_as_uint(y.x), __float_as_uint(y.y), T0, T1);
    SB();
    STAMP(8);
    __builtin_amdgcn_s_barrier();
    SB();
    if (w < 3) {
      u32x4 da[8]; u32 db[8];
      const rsrc_t rd = mk(a.wd, F * D * 2);
      const u32 od = (u32)tid * (8u * D * 2u) + 160u * xg + 20u * c8;
#pragma unroll
      for (int n = 0; n < 8; ++n) {
        da[n] = __builtin_amdgcn_raw_buffer_load_b128(rd, od, n * D * 2, 0);
        db[n] = __builtin_amdgcn_raw_buffer_load_b32(rd, od + 16u, n * D * 2, 0);
      }
      u32 dp[4][10];
#pragma unroll
      for (int n = 0; n < 4; ++n) {
#pragma unroll
        for (int e = 0; e < 4; ++e) { dp[n][2 * e] = zip_lo(da[2 * n][e], da[2 * n + 1][e]); dp[n][2 * e + 1] = zip_hi(da[2 * n][e], da[2 * n + 1][e]); }
        dp[n][8] = zip_lo(db[2 * n], db[2 * n + 1]); dp[n][9] = zip_hi(db[2 * n], db[2 * n + 1]);
#pragma unroll
        for (int e = 0; e < 10; ++e) asm volatile("" : "+v"(dp[n][e]));
      }
      STAMP(10);
      u32 off[2] = {WS_X4 + (u32)xg * X4_REP + (u32)tid * 128u, WS_X4 + (u32)xg * X4_REP + (u32)tid * 128u + 16u}, d0[2], d1[2];
      get_recs<2, SLP_C>(ws, off, T0, T1, d0, d1, dead);
      STAMP(11);
      float c[10], c5[5];
#pragma unroll
      for (int e = 0; e < 10; ++e) c[e] = dot2(dp[3][e], d1[1], dot2(dp[2][e], d0[1], dot2(dp[1][e], d1[0], dot2(dp[0][e], d0[0], 0.f))));
#pragma unroll
      for (int e = 0; e < 5; ++e) c5[e] = tsum32(c[e], c[e + 5]);
      const float v0 = row_sum(tsum16(c5[0], c5[1])), v1 = row_sum(tsum16(c5[2], c5[3])), v2 = row_sum(tsum16(c5[4], c5[4]));
      if ((lane & 15) == 0) {
        LAS float* dn = lp<float>(L, L_DN) + w * 16 + 5 * (lane >> 5);
        const int b4 = (lane >> 4) & 1;
        dn[b4] = v0; dn[2 + b4] = v1; dn[4] = v2;
      }
    } else if (w == 3) {
      const int cp = lane & 7;
      u32 off[1] = {WS_X3 + (u32)xg * X3_REP + (u32)(r * 320 + 40 * xg + 5 * c8 + (cp < 5 ? cp : 0)) * 16u}, d0[1], d1[1];
      const u32 xv = __builtin_amdgcn_raw_buffer_load_b32(rx, (40 * xg + 5 * c8 + (cp < 5 ? cp : 0)) * 4, 0, 0);
      __builtin_amdgcn_s_sleep(PRE);
      get_recs<1, 8>(ws, off, T0, T1, d0, d1, dead);
      float s0 = __uint_as_float(d0[0]), s1 = __uint_as_float(d1[0]);
      s0 = tsum32(s0, s0); s1 = tsum32(s1, s1); s0 = tsum16(s0, s0); s1 = tsum16(s1, s1); s0 += dpp<R8>(s0); s1 += dpp<R8>(s1);
      const u32 at = pk_bf16(s0, s1);
      if (lane < 5) lp<u32>(L, L_H)[lane] = pk_bf16(lo_f(xv) + lo_f(at), hi_f(xv) + hi_f(at));
    }
    bar();
    if (tid < 10) {
      const float s = rbf((lp<float>(L, L_DN)[tid] + lp<float>(L, L_DN)[16 + tid]) + lp<float>(L, L_DN)[32 + tid]);
      a.out[80 * xg + 10 * c8 + tid] = (u16)pk_bf16(lo_f(lp<u16>(L, L_H)[tid]) + s, 0.f);
    }
  } else {
    bar();
    u32 gp[16];
    {
      const rsrc_t rg = mk(a.wg, D * F * 2), ru = mk(a.wu, D * F * 2);
      const u32 og = (u32)tid * (2u * F * 2u) + 384u * xg + 16u * j;
      const u32x4 A = __builtin_amdgcn_raw_buffer_load_b128(rg, og, 0, 0), B = __builtin_amdgcn_raw_buffer_load_b128(rg, og, F * 2, 0);
      const u32x4 C = __builtin_amdgcn_raw_buffer_load_b128(ru, og, 0, 0), E = __builtin_amdgcn_raw_buffer_load_b128(ru, og, F * 2, 0);
#pragma unroll
      for (int e = 0; e < 4; ++e) {
        gp[2 * e] = zip_lo(A[e], B[e]); gp[2 * e + 1] = zip_hi(A[e], B[e]);
        gp[8 + 2 * e] = zip_lo(C[e], E[e]); gp[8 + 2 * e + 1] = zip_hi(C[e], E[e]);
      }
#pragma unroll
      for (int e = 0; e < 16; ++e) asm volatile("" : "+v"(gp[e]));
    }
    STAMP(8);
    if (w == SVC) {
      u32 off[1] = {WS_X3 + (u32)xg * X3_REP + (u32)(r * 320 + 40 * j8 + 39) * 16u}, d0[1], d1[1];
      get_recs<1, 4>(ws, off, T0, T1, d0, d1, dead);
    }
    bar();
    u32 hp;
    {
      u32 off[8], d0[8], d1[8];
#pragma unroll
      for (int k = 0; k < 8; ++k) off[k] = WS_X3 + (u32)xg * X3_REP + (u32)(k * 320 + tid) * 16u;
      get_recs<8, SLP_B>(ws, off, T0, T1, d0, d1, dead);
      auto sum8 = [](const u32 (&d)[8]) {
        float t[4];
#pragma unroll
        for (int k = 0; k < 4; ++k) t[k] = __uint_as_float(d[k]) + __uint_as_float(d[k + 4]);
        return (t[0] + t[2]) + (t[1] + t[3]);
      };
      const u32 at = pk_bf16(sum8(d0), sum8(d1));
      hp = pk_bf16(lo_f(xme) + lo_f(at), hi_f(xme) + hi_f(at));
    }
    STAMP(9);
    const float h0 = lo_f(hp), h1 = hi_f(hp);
    {
      const float mw = wave_sum(h0 + h1) * (1.f / 128.f);
      const float m2 = wave_sum((h0 - mw) * (h0 - mw) + (h1 - mw) * (h1 - mw));
      if (lane == 0) lp<f32x2>(L, L_ST)[w] = f32x2{mw, m2};
    }
    bar();
    if (b == 0 && tid == 0) {
      const u64 e1 = (((u64)ev.y << 32) | ev.x) + 1ull;
      __builtin_amdgcn_raw_buffer_store_b64(u32x2{(u32)e1, (u32)(e1 >> 32)}, ws, WS_E, 0, AUX_SC1);
    }
    f32x2 st[5];
#pragma unroll
    for (int ww = 0; ww < 5; ++ww) st[ww] = lp<f32x2>(L, L_ST)[ww];
    const float mean = (((st[0].x + st[1].x) + (st[2].x + st[3].x)) + st[4].x) * 0.2f;
    float m2 = 0.f;
#pragma unroll
    for (int ww = 0; ww < 5; ++ww) m2 += st[ww].y + 128.f * (st[ww].x - mean) * (st[ww].x - mean);
    const float var = rbf(m2 * (1.f / D));
    const float mb = rbf(mean), inv = __builtin_amdgcn_rcpf(rbf(__builtin_amdgcn_sqrtf(rbf(var + EPS))));
    const u32 dd = pk_bf16(h0 - mb, h1 - mb);
    const u32 hn = pk_bf16(lo_f(dd) * inv, hi_f(dd) * inv);
    float g[16];
#pragma unroll
    for (int e = 0; e < 16; ++e) g[e] = dot2(gp[e], hn, 0.f);
    float g8[8], g4[4], g2[2];
#pragma unroll
    for (int e = 0; e < 8; ++e) g8[e] = tsum32(g[e], g[e + 8]);
#pragma unroll
    for (int e = 0; e < 4; ++e) g4[e] = tsum16(g8[e], g8[e + 4]);
    const bool b3 = (lane & 8) != 0, b2 = (lane & 4) != 0;
#pragma unroll
    for (int e = 0; e < 2; ++e) g2[e] = (b3 ? g4[e + 2] : g4[e]) + dpp<R8>(b3 ? g4[e] : g4[e + 2]);
    float g1 = (b2 ? g2[1] : g2[0]) + dpp<X7>(b2 ? g2[0] : g2[1]);
    g1 += dpp<X2>(g1); g1 += dpp<X1>(g1);
    if ((lane & 3) == 0) lp<float>(L, L_GU)[w * 16 + (lane >> 2)] = g1;
    bar();
    if (w == 0) {
      float gs = 0.f, us = 0.f;
#pragma unroll
      for (int ww = 0; ww < 5; ++ww) { gs += lp<float>(L, L_GU)[ww * 16 + j8]; us += lp<float>(L, L_GU)[ww * 16 + 8 + j8]; }
      const u32 gu = pk_bf16(gs, us);
      const float sg = rbf(__builtin_amdgcn_rcpf(1.f + __builtin_amdgcn_exp2f(-1.4426950408889634f * lo_f(gu))));
      const float f = sg * hi_f(gu);
      const u32 d0 = pk_bf16(dpp<0x00>(f), dpp<0x55>(f)), d1 = pk_bf16(dpp<0xAA>(f), dpp<0xFF>(f));
      put_rec(ws, (lane & 3) == 0 ? WS_X4 + (u32)r * X4_REP + (u32)(xg * 24 + j) * 128u + (u32)(lane & 4) * 4u : OOB, d0, d1, T0, T1);
    }
    STAMP(10);
  }
  if (PADE) pad(PADE);
  STAMP(12);
  asm volatile(".Lfb_end:");
}
}

extern "C" void kernel_launch(void* const* d_in, const int* in_sizes, int n_in, void* d_out, int out_size, void* d_ws, size_t ws_size,
                              hipStream_t stream) {
  Args a;
  a.x = (const u16*)d_in[0]; a.k = (const u16*)d_in[1]; a.v = (const u16*)d_in[2]; a.wq = (const u16*)d_in[3]; a.wo = (const u16*)d_in[4];
  a.wg = (const u16*)d_in[6]; a.wu = (const u16*)d_in[7]; a.wd = (const u16*)d_in[8];
  a.out = (u16*)d_out; a.ws = (unsigned char*)d_ws;
  hipLaunchKernelGGL(fused_block, dim3(256), dim3(NTHR), 0, stream, a);
}
```

```cpp
#include <hip/hip_runtime.h>
#include <hip/hip_bf16.h>
#include <stdint.h>

#define STAMPS 0
#define PACE 1340
#define RING 6
#define PFT 1
#define BG 2
#define SENT_A 1
#define SENT_B 1
#define SLP_A 0
#define SLP_B 0
#define SLP_C 2
#define NOPF 0
#define DUTY0 40
#define DUTY1 10
#define DUTY2 32
#define PFWAIT 1
#define CPF 3
#define CPB 14
#define TCH 3
#define PADQ 0
#define PADV 0
#define PADE 0
#define SKM 0
#define QDELAY 32
#define PRE 10
#define PACE_BURST 5000

typedef unsigned u32;
typedef unsigned long long u64;
typedef unsigned short u16;
typedef u32 u32x2 __attribute__((ext_vector_type(2)));
typedef u32 u32x3 __attribute__((ext_vector_type(3)));
typedef u32 u32x4 __attribute__((ext_vector_type(4)));
typedef float f32x2 __attribute__((ext_vector_type(2)));
typedef float f32x4 __attribute__((ext_vector_type(4)));
typedef __bf16 bf16x2 __attribute__((ext_vector_type(2)));
typedef __amdgpu_buffer_rsrc_t rsrc_t;
#define LAS __attribute__((address_space(3)))

namespace {
constexpr int D = 640, H = 32, SK = 32768, DH = 192, F = 1536;
constexpr u32 HEADB = (u32)SK * DH * 2;
constexpr int NTHR = 320, SVC = 4;
constexpr u32 OOB = 0x80000000u;
constexpr int AUX_NT = 2, AUX_SC1 = 16;
constexpr u32 WS_E = 0, WS_X1 = 4096, WS_X2 = WS_X1 + 4 * 8 * 32 * 128, WS_X0 = WS_X2 + 4 * 8 * 32 * 1536, WS_XC = WS_X0 + 8 * 32 * 1536,
              WS_XO = WS_XC + 8 * 24 * 640, WS_XT = WS_XO + 8 * 24 * 128, WS_X3 = WS_XT + 256 * 128, X3_REP = 8 * 320 * 16, WS_X4 = WS_X3 + 8 * X3_REP, X4_REP = 192 * 128, WS_BYTES = WS_X4 + 8 * X4_REP;
constexpr u32 WS_STAMP = 4u << 20;
static_assert(WS_BYTES <= WS_STAMP && WS_XC % 128 == 0 && WS_X3 % 128 == 0 && WS_X4 % 128 == 0, "ws");
constexpr int L_Q = 0, L_Y = 1536, L_FLAG = 2048, L_ST = 2176, L_GU = 2240, L_DN = 2560, L_H = 2752, L_OB = 3072, L_RED = 5120,
              L_S = 8192, L_PART = 16384, L_WO = 28672, L_WP = 59392, LDS_BYTES = L_WP + 30720;
constexpr int F_K = 0, F_P = 4, F_V = 8, F_O = 12, F_DEAD = 13, F_Q = 14, F_L = 15, F_BLK = 16;
constexpr u32 SPIN_G = 1u << 17, SPIN_L = 1u << 21;

struct Args {
  const u16 *x, *k, *v, *wq, *wo, *wg, *wu, *wd;
  u16* out;
  unsigned char* ws;
};

__device__ __forceinline__ float dot2(u32 a, u32 b, float c) {
  return __builtin_amdgcn_fdot2_f32_bf16(__builtin_bit_cast(bf16x2, a), __builtin_bit_cast(bf16x2, b), c, false);
}
__device__ __forceinline__ u32 pk_bf16(float lo, float hi) {
  bf16x2 r = {(__bf16)lo, (__bf16)hi};
  return __builtin_bit_cast(u32, r);
}
__device__ __forceinline__ float lo_f(u32 a) { return __uint_as_float(a << 16); }
__device__ __forceinline__ float hi_f(u32 a) { return __uint_as_float(a & 0xffff0000u); }
__device__ __forceinline__ float rbf(float a) { return lo_f(pk_bf16(a, a)); }
__device__ __forceinline__ u32 zip_lo(u32 a, u32 b) { return __builtin_amdgcn_perm(b, a, 0x05040100u); }
__device__ __forceinline__ u32 zip_hi(u32 a, u32 b) { return __builtin_amdgcn_perm(b, a, 0x07060302u); }
template <int CTRL> __device__ __forceinline__ float dpp(float x) {
  return __builtin_bit_cast(float, __builtin_amdgcn_mov_dpp(__builtin_bit_cast(int, x), CTRL, 0xf, 0xf, true));
}
constexpr int X1 = 0xB1, X2 = 0x4E, X7 = 0x141, R1 = 0x121, R2 = 0x122, R4 = 0x124, R8 = 0x128;
__device__ __forceinline__ float tsum32(float a, float b) {
  auto s = __builtin_amdgcn_permlane32_swap(__float_as_uint(a), __float_as_uint(b), false, false);
  return __uint_as_float(s[0]) + __uint_as_float(s[1]);
}
__device__ __forceinline__ float tsum16(float a, float b) {
  auto s = __builtin_amdgcn_permlane16_swap(__float_as_uint(a), __float_as_uint(b), false, false);
  return __uint_as_float(s[0]) + __uint_as_float(s[1]);
}
__device__ __forceinline__ float row_sum(float x) { x += dpp<R8>(x); x += dpp<X7>(x); x += dpp<X2>(x); x += dpp<X1>(x); return x; }
__device__ __forceinline__ float row_max(float x) {
  x = fmaxf(x, dpp<R8>(x)); x = fmaxf(x, dpp<X7>(x)); x = fmaxf(x, dpp<X2>(x)); x = fmaxf(x, dpp<X1>(x)); return x;
}
__device__ __forceinline__ float wave_sum(float x) { x = row_sum(x); x = tsum16(x, x); return tsum32(x, x); }
__device__ __forceinline__ float wave_max(float x) {
  x = row_max(x);
  auto s = __builtin_amdgcn_permlane16_swap(__float_as_uint(x), __float_as_uint(x), false, false);
  x = fmaxf(__uint_as_float(s[0]), __uint_as_float(s[1]));
  auto t = __builtin_amdgcn_permlane32_swap(__float_as_uint(x), __float_as_uint(x), false, false);
  return fmaxf(__uint_as_float(t[0]), __uint_as_float(t[1]));
}
__device__ __forceinline__ rsrc_t mk(const void* p, u32 bytes) { return __builtin_amdgcn_make_buffer_rsrc((void*)p, 0, bytes, 0x00020000); }
__device__ __forceinline__ void bar() {
  asm volatile("s_waitcnt lgkmcnt(0)" ::: "memory");
  __builtin_amdgcn_s_barrier();
  asm volatile("" ::: "memory");
}
__device__ __forceinline__ void touch2(const void* p, const void* q) {
  u32 d0, d1;
  asm volatile("s_load_dword %0, %2, 0x0\n\ts_load_dword %1, %3, 0x0\n\ts_waitcnt lgkmcnt(0)" : "=&s"(d0), "=&s"(d1) : "s"(p), "s"(q) : "memory");
}
__device__ __forceinline__ u64 rt() { return __builtin_amdgcn_s_memrealtime(); }
#define SB() __builtin_amdgcn_sched_barrier(0)

template <typename T> __device__ __forceinline__ LAS T* lp(LAS unsigned char* l, int off) { return (LAS T*)(l + off); }

__device__ __forceinline__ void put_rec(rsrc_t ws, u32 off, u32 d0, u32 d1, u32 T0, u32 T1) {
  __builtin_amdgcn_raw_buffer_store_b128(u32x4{d0, d1, d0 ^ T0, d1 ^ T1}, ws, off, 0, AUX_SC1);
}
template <int N, int SLEEP>
__device__ __forceinline__ void get_recs(rsrc_t ws, const u32 (&off)[N], u32 T0, u32 T1, u32 (&d0)[N], u32 (&d1)[N], volatile LAS u32* dead) {
#pragma unroll 1
  for (u32 spin = 0;; ++spin) {
    asm volatile("" ::: "memory");
    u32x4 t[N];
#pragma unroll
    for (int k = 0; k < N; ++k) t[k] = __builtin_amdgcn_raw_buffer_load_b128(ws, off[k], 0, AUX_SC1);
    SB();
    u32 bad = 0u;
#pragma unroll
    for (int k = 0; k < N; ++k) { bad |= (t[k].x ^ t[k].z ^ T0) | (t[k].y ^ t[k].w ^ T1); d0[k] = t[k].x; d1[k] = t[k].y; }
    if (!__builtin_amdgcn_ballot_w64(bad != 0u)) break;
    if (SLEEP) __builtin_amdgcn_s_sleep(SLEEP);
    if ((spin & 255u) == 255u) { if (*dead) break; if (spin > SPIN_G) { *dead = 1u; break; } }
  }
}
template <int SLEEP>
__device__ __forceinline__ void wait_flag(volatile LAS u32* f, u32 want, volatile LAS u32* dead) {
  for (u32 spin = 0; (u32)__builtin_amdgcn_readfirstlane(*f) < want; ++spin) {
    if (SLEEP) __builtin_amdgcn_s_sleep(SLEEP);
    if ((spin & 255u) == 255u) { if (*dead) break; if (spin > SPIN_L) { *dead = 1u; break; } }
  }
  asm volatile("" ::: "memory");
}
__device__ __forceinline__ void signal_add(LAS u32* f, int lane) {
  asm volatile("s_waitcnt lgkmcnt(0)" ::: "memory");
  if (lane == 0) __hip_atomic_fetch_add(f, 1u, __ATOMIC_RELAXED, __HIP_MEMORY_SCOPE_WORKGROUP);
}

#define STAMP(i) do { } while (0)

template <int G, int SLEEP>
__device__ __forceinline__ void gather8(rsrc_t ws, LAS unsigned char* L, u32 first, int lane, u32 T0, u32 T1, float (&s)[G][3]) {
  u32 off[12 * G], d0[12 * G], d1[12 * G];
  const u32 base = first + (u32)((lane >> 5) * 96 + (lane & 31)) * 16u;
#pragma unroll
  for (int n = 0; n < 4 * G; ++n)
#pragma unroll
    for (int pr = 0; pr < 3; ++pr) off[n * 3 + pr] = base + (u32)(2 * n * 96 + 32 * pr) * 16u;
  get_recs<12 * G, SLEEP>(ws, off, T0, T1, d0, d1, lp<u32>(L, L_FLAG) + F_DEAD);
#pragma unroll
  for (int g = 0; g < G; ++g)
#pragma unroll
    for (int pr = 0; pr < 3; ++pr) {
      const int k = 12 * g + pr;
      float a = (__uint_as_float(d0[k]) + __uint_as_float(d0[k + 3])) + (__uint_as_float(d0[k + 6]) + __uint_as_float(d0[k + 9]));
      float b = (__uint_as_float(d1[k]) + __uint_as_float(d1[k + 3])) + (__uint_as_float(d1[k + 6]) + __uint_as_float(d1[k + 9]));
      s[g][pr] = tsum32(a, b);
    }
}
template <int G, int SLEEP>
__device__ __forceinline__ void gather_o(rsrc_t ws, LAS unsigned char* L, int t, int xg, int vw, int lane, u32 T0, u32 T1) {
  float s[G][3];
  gather8<G, SLEEP>(ws, L, WS_X2 + (u32)(((t * 8 + xg) * 32 + 8 * vw) * 96) * 16u, lane, T0, T1, s);
#pragma unroll
  for (int g = 0; g < G; ++g)
#pragma unroll
    for (int pr = 0; pr < 3; ++pr) lp<float>(L, L_RED)[(((vw + g) * 3 + pr) * 32 + (lane & 31)) * 2 + (lane >> 5)] = s[g][pr];
}
__device__ __forceinline__ void flush24(const float (&acc)[24], LAS float* dst, int lane) {
#pragma unroll
  for (int i = 0; i < 3; ++i) {
    float s1[4], s2[2];
#pragma unroll
    for (int e = 0; e < 4; ++e) s1[e] = tsum32(acc[i * 8 + e], acc[i * 8 + e + 4]);
#pragma unroll
    for (int e = 0; e < 2; ++e) s2[e] = tsum16(s1[e], s1[e + 2]);
    const bool b3 = (lane & 8) != 0;
    const float keep = b3 ? s2[1] : s2[0], send = b3 ? s2[0] : s2[1];
    dst[64 * i + 8 * (lane & 7) + (lane >> 3)] = keep + dpp<R8>(send);
  }
}
__device__ __forceinline__ void publish192(rsrc_t ws, const LAS f32x2* src, u32 first, int lane, u32 T0, u32 T1) {
  const LAS f32x2* p0 = src + lane; const LAS f32x2* p1 = src + 64 + (lane & 31);
  const f32x2 s0 = (p0[0] + p0[96]) + (p0[192] + p0[288]), s1 = (p1[0] + p1[96]) + (p1[192] + p1[288]);
  put_rec(ws, first + (u32)lane * 16u, __float_as_uint(s0.x), __float_as_uint(s0.y), T0, T1);
  put_rec(ws, lane < 32 ? first + 1024u + (u32)lane * 16u : OOB, __float_as_uint(s1.x), __float_as_uint(s1.y), T0, T1);
}
__device__ __forceinline__ void pack_wo(LAS unsigned char* L, int vw, int lane) {
  const LAS u32* src = lp<u32>(L, L_WO) + (lane >> 3) * 960 + 8 * vw + (lane & 7);
#pragma unroll
  for (int m = 0; m < 12; m += 2) {
    const u32 A = src[80 * m], B = src[80 * m + 40], C = src[80 * m + 80], E = src[80 * m + 120];
    lp<u32x4>(L, L_WP)[(vw * 6 + m / 2) * 64 + lane] = u32x4{zip_lo(A, B), zip_hi(A, B), zip_lo(C, E), zip_hi(C, E)};
  }
}
__device__ __forceinline__ void load_wo(LAS unsigned char* L, int vw, int lane, u32x4 (&wp)[6]) {
#pragma unroll
  for (int m = 0; m < 6; ++m) wp[m] = lp<u32x4>(L, L_WP)[(vw * 6 + m) * 64 + lane];
}
__device__ __forceinline__ void finish_o(LAS unsigned char* L, LAS u32* ob, int lane) {
  const LAS f32x4* rp = lp<f32x4>(L, L_RED) + (lane < 48 ? lane : 0);
  const f32x4 s = (rp[0] + rp[48]) + (rp[96] + rp[144]);
  if (lane < 48) ((LAS u32x2*)ob)[lane] = u32x2{pk_bf16(s.x, s.y), pk_bf16(s.z, s.w)};
  asm volatile("s_waitcnt lgkmcnt(0)" ::: "memory");
}
__device__ __forceinline__ f32x2 gemv16(const LAS u32* ob, const u32x4 (&wp)[6], int lane) {
  const LAS u32x4* op = (const LAS u32x4*)ob + (lane >> 3) * 3;
  const u32x4 o0 = op[0], o1 = op[1], o2 = op[2];
  float y[4] = {0.f, 0.f, 0.f, 0.f};
#pragma unroll
  for (int m = 0; m < 12; ++m) {
    const u32 o = m < 4 ? o0[m & 3] : m < 8 ? o1[m & 3] : o2[m & 3];
    y[2 * (m & 1)] = dot2(wp[m >> 1][2 * (m & 1)], o, y[2 * (m & 1)]); y[2 * (m & 1) + 1] = dot2(wp[m >> 1][2 * (m & 1) + 1], o, y[2 * (m & 1) + 1]);
  }
  float y0 = y[0] + y[2], y1 = y[1] + y[3];
  y0 += dpp<R8>(y0); y1 += dpp<R8>(y1);
  y0 = tsum16(y0, y0); y1 = tsum16(y1, y1);
  return f32x2{tsum32(y0, y0), tsum32(y1, y1)};
}

__global__ void __launch_bounds__(NTHR, 1) fused_block(Args a) {
  __shared__ __attribute__((aligned(16))) unsigned char lds_raw[LDS_BYTES];
  LAS unsigned char* L = (LAS unsigned char*)lds_raw;
  const int tid = threadIdx.x, lane = tid & 63, w = __builtin_amdgcn_readfirstlane(tid >> 6);
  const int b = blockIdx.x, xg = b & 7, j = b >> 3;
  const int r = lane >> 3, j8 = lane & 7;
  const int tj = j >> 3, c8 = j & 7;
  volatile LAS u32* FL = lp<u32>(L, L_FLAG);
  volatile LAS u32* dead = FL + F_DEAD;
  STAMP(0);
  const u32 tstart = (u32)rt();
  auto pad = [&](u32 ticks) { while ((u32)rt() - tstart < ticks) __builtin_amdgcn_s_sleep(1); };
  const rsrc_t ws = mk(a.ws, WS_BYTES);
  const rsrc_t rx = mk(a.x, D * 2), rq = mk(a.wq, D * H * DH * 2);

  const float EPS = 1e-5f;
  u64 pc0; u32 clen;
  asm volatile("s_getpc_b64 %0\n.Lfb_begin:\n\ts_mov_b32 %1, .Lfb_end-.Lfb_begin" : "=s"(pc0), "=s"(clen));
  auto code_touch = [&](int piece) {
    const u32 v = __builtin_amdgcn_raw_buffer_load_b32(mk((const void*)pc0, clen), (u32)piece * 1024u + (u32)lane * 16u, 0, 0);
    asm volatile("" :: "v"(v));
  };
  u32 eoff = WS_E; asm volatile("" : "+v"(eoff));
  const u32 voff = (u32)r * 384u + (u32)j8 * 16u;
  const u32 sbase = (u32)xg * HEADB + (u32)j * 24576u + (u32)w * 6144u;
  auto step_off = [&](int n) -> u32 { return sbase + (u32)(n >> 4) * (8u * HEADB) + (u32)(n & 15) * (32u * 24576u); };
  u32x2 ev; u32 xme, T0, T1;
  auto make_tag = [&]() {
    u64 z = (((u64)ev.y << 32) | ev.x) + 0x9E3779B97F4A7C15ull;
    z = (z ^ (z >> 30)) * 0xBF58476D1CE4E5B9ull; z = (z ^ (z >> 27)) * 0x94D049BB133111EBull; z ^= z >> 31;
    T0 = __builtin_amdgcn_readfirstlane((u32)z) | 1u; T1 = __builtin_amdgcn_readfirstlane((u32)(z >> 32)) | 1u;
  };

  if (w == SVC) {
    ev = __builtin_amdgcn_raw_buffer_load_b64(ws, eoff, 0, AUX_SC1);
    xme = __builtin_amdgcn_raw_buffer_load_b32(rx, tid * 4, 0, 0);
    if (lane < 32) FL[lane] = 0u;
    if (CPF & 1) code_touch(j);
    if ((TCH & 1) && j >= 26) { const char* p = (const char*)a.k + (size_t)xg * HEADB + (size_t)(j - 25) * 2097152 - 64; touch2(p, p); }
    make_tag();
    put_rec(ws, lane == 0 ? WS_XT + (u32)b * 128u : OOB, tstart, tstart, T0, T1);
    STAMP(1);
    bar();
    publish192(ws, lp<f32x2>(L, L_PART), WS_X0 + (u32)((xg * 32 + j) * 96) * 16u, lane, T0, T1);
    __builtin_amdgcn_s_sleep(QDELAY);
#pragma unroll 1
    for (int t = 0; t < 4; ++t) {
      float s[1][3];
      gather8<1, 4>(ws, L, WS_X0 + (u32)((xg * 32 + 8 * t) * 96) * 16u, lane, T0, T1, s);
#pragma unroll
      for (int pr = 0; pr < 3; ++pr) lp<u16>(L, L_Q)[t * 192 + (32 * pr + (lane & 31)) * 2 + (lane >> 5)] = (u16)pk_bf16(s[0][pr], 0.f);
      if (t == 0) {
        STAMP(2); bar();
        if (TCH & 2) {
          auto at = [&](int n) -> const char* {
            if (n < 49) { const int sl = n / 7 + 1, pt = n % 7;
              return (const char*)(sl < 4 ? a.k : a.v) + (size_t)(8 * (sl & 3) + xg) * HEADB + (pt ? (size_t)pt * 2097152 - 64 : 0); }
            const int m = n - 49, e = (m & 1) * (D * F * 2 - 64);
            return m < 2 ? (const char*)a.wg + e : m < 4 ? (const char*)a.wu + e : m < 6 ? (const char*)a.wd + e : m < 8 ? (const char*)a.out : (const char*)a.ws + (m & 1) * (WS_BYTES - 64);
          };
          touch2(at(j), at(j + 32));
        }
      }
      __builtin_amdgcn_s_sleep(64);
    }
    signal_add((LAS u32*)FL + F_Q, lane);
#pragma unroll 1
    for (int t = 0; t < 4; ++t) {
      wait_flag<8>(FL + F_K + t, 4u, dead);
      if (t == 0) {
#pragma unroll 1
        for (int vw = 0; vw < 5; ++vw) pack_wo(L, vw, lane);
      }
      LAS u32x4* sp = lp<u32x4>(L, L_S + ((lane >> 4) * 4 + t) * 512 + (lane & 15) * 32);
      const u32x4 s0 = sp[0], s1 = sp[1];
      float s[16];
#pragma unroll
      for (int e = 0; e < 4; ++e) { s[2 * e] = lo_f(s0[e]); s[2 * e + 1] = hi_f(s0[e]); s[8 + 2 * e] = lo_f(s1[e]); s[8 + 2 * e + 1] = hi_f(s1[e]); }
      float m = s[0];
#pragma unroll
      for (int e = 1; e < 16; ++e) m = fmaxf(m, s[e]);
      m = wave_max(m);
      const float L2E = 1.4426950408889634f;
      float l = 0.f;
#pragma unroll
      for (int e = 0; e < 16; ++e) l += __builtin_amdgcn_exp2f((s[e] - m) * L2E);
      l = wave_sum(l);
      put_rec(ws, lane == 0 ? WS_X1 + (u32)((t * 8 + xg) * 32 + j) * 128u : OOB, __float_as_uint(m), __float_as_uint(l), T0, T1);
      u32 off[1] = {WS_X1 + (u32)((t * 8 + xg) * 32 + (lane & 31)) * 128u}, d0[1], d1[1];
      __builtin_amdgcn_s_sleep(32);
      get_recs<1, 32>(ws, off, T0, T1, d0, d1, dead);
      const float mj = __uint_as_float(d0[0]), lj = __uint_as_float(d1[0]);
      const float M = wave_max(mj);
      float Ls = row_sum(lj * __builtin_amdgcn_exp2f((mj - M) * L2E));
      Ls = tsum16(Ls, Ls);
      Ls = __uint_as_float(__builtin_amdgcn_readfirstlane(__float_as_uint(Ls)));
      const float inv = 1.f / Ls;
      u32x4 p0, p1;
#pragma unroll
      for (int e = 0; e < 4; ++e) {
        p0[e] = pk_bf16(__builtin_amdgcn_exp2f((s[2 * e] - M) * L2E) * inv, __builtin_amdgcn_exp2f((s[2 * e + 1] - M) * L2E) * inv);
        p1[e] = pk_bf16(__builtin_amdgcn_exp2f((s[8 + 2 * e] - M) * L2E) * inv, __builtin_amdgcn_exp2f((s[8 + 2 * e + 1] - M) * L2E) * inv);
      }
      sp[0] = p0; sp[1] = p1;
      signal_add((LAS u32*)FL + F_P + t, lane);
    }
#pragma unroll 1
    for (int t = 0; t < 3; ++t) {
      wait_flag<8>(FL + F_V + t, 4u, dead);
      if (t == 2) STAMP(3);
      publish192(ws, lp<f32x2>(L, L_PART + t * 3072), WS_X2 + (u32)(((t * 8 + xg) * 32 + j) * 96) * 16u, lane, T0, T1);
      if (tj == t) {
        __builtin_amdgcn_s_sleep(DUTY0);
        {
          const int rc = lane & 15;
          u32 off[8], d0[8], d1[8];
#pragma unroll
          for (int n = 0; n < 8; ++n) off[n] = WS_X2 + (u32)(((t * 8 + xg) * 32 + 4 * n + (lane >> 4)) * 96 + 12 * c8 + (rc < 12 ? rc : 0)) * 16u;
          get_recs<8, 32>(ws, off, T0, T1, d0, d1, dead);
          if (t == 2) STAMP(4);
          float s0 = 0.f, s1 = 0.f;
#pragma unroll
          for (int n = 0; n < 8; ++n) { s0 += __uint_as_float(d0[n]); s1 += __uint_as_float(d1[n]); }
          s0 = tsum16(s0, s0); s1 = tsum16(s1, s1); s0 = tsum32(s0, s0); s1 = tsum32(s1, s1);
          const u32 op = pk_bf16(s0, s1), on = __float_as_uint(dpp<X1>(__uint_as_float(op)));
          put_rec(ws, (lane < 12 && !(lane & 1)) ? WS_XO + (u32)(xg * 24 + j) * 128u + (u32)lane * 8u : OOB, op, on, T0, T1);
        }
        __builtin_amdgcn_s_sleep(DUTY1);
        {
          const int l6 = (lane * 43) >> 8;
          u32 off[1] = {WS_XO + (lane < 48 ? (u32)(xg * 24 + 8 * t + l6) * 128u + (u32)(lane - 6 * l6) * 16u : (u32)(xg * 24 + 8 * t) * 128u)}, d0[1], d1[1];
          get_recs<1, 16>(ws, off, T0, T1, d0, d1, dead);
          if (lane < 48) lp<u32x2>(L, L_OB)[lane] = u32x2{d0[0], d1[0]};
          asm volatile("s_waitcnt lgkmcnt(0)" ::: "memory");
        }
        if (t == 2) STAMP(5);
#pragma unroll 1
        for (int vw = 0; vw < 5; ++vw) {
          u32x4 wp[6];
          load_wo(L, vw, lane, wp);
          f32x2 y = gemv16(lp<u32>(L, L_OB), wp, lane);
          if (t > 0) y += lp<f32x2>(L, L_Y)[8 * vw + j8];
          put_rec(ws, lane < 8 ? WS_XC + (u32)(xg * 24 + j) * 640u + (u32)(8 * vw + lane) * 16u : OOB, __float_as_uint(y.x), __float_as_uint(y.y), T0, T1);
        }
      }
      if (tj == t + 1) {
        __builtin_amdgcn_s_sleep(127); __builtin_amdgcn_s_sleep(DUTY2);
        u32 off[1] = {WS_XC + (u32)(xg * 24 + j - 8) * 640u + (u32)(lane < 40 ? lane : 0) * 16u}, d0[1], d1[1];
        get_recs<1, 32>(ws, off, T0, T1, d0, d1, dead);
        if (lane < 40) lp<u32x2>(L, L_Y)[lane] = u32x2{d0[0], d1[0]};
      }
      if (t == 2) STAMP(13);
    }
    if ((CPF & 2) && tj < 2) { wait_flag<8>(FL + F_BLK, 1u, dead); code_touch(2 * j); code_touch(2 * j + 1); }
  } else {
    u32 xs[5]; u16 xr[3]; u32x4 wqv[3][3]; u32x4 ring[RING][2];
#pragma unroll
    for (int m = 0; m < 5; ++m) xs[m] = __builtin_amdgcn_raw_buffer_load_b32(rx, (lane + 64 * m) * 4, 0, 0);
    {
      const u32 row = (u32)(80 * (j & 7) + 20 * w + r);
      const u32 o = row * (u32)(H * DH * 2) + (u32)(8 * (j >> 3) + xg) * 384u + (u32)j8 * 16u;
#pragma unroll
      for (int g = 0; g < 3; ++g) {
        const u32 og = (g < 2 || r < 4) ? o : OOB, ox = (g < 2 || r < 4) ? row * 2u : OOB;
#pragma unroll
        for (int i = 0; i < 3; ++i) wqv[g][i] = __builtin_amdgcn_raw_buffer_load_b128(rq, og + i * 128, g * 8 * (H * DH * 2), AUX_NT);
        xr[g] = __builtin_amdgcn_raw_buffer_load_b16(rx, ox, g * 16, 0);
      }
    }
    ev = __builtin_amdgcn_raw_buffer_load_b64(ws, eoff, 0, AUX_SC1);
    xme = __builtin_amdgcn_raw_buffer_load_b32(rx, tid * 4, 0, 0);
    SB();
    auto issue = [&](int slot, int n, int i) {
      const rsrc_t rs = mk(n < 64 ? a.k : a.v, n < 128 ? HEADB * H : 0u);
      const u32 so = step_off(n & 63);
      ring[slot][0] = __builtin_amdgcn_raw_buffer_load_b128(rs, voff + i * 128, so, AUX_NT);
      ring[slot][1] = __builtin_amdgcn_raw_buffer_load_b128(rs, voff + 3072 + i * 128, so, AUX_NT);
      SB();
    };
#pragma unroll
    for (int u = 0; u < RING; ++u) issue(u, u / 3, u % 3);
    float acc[24];
    {
      float s = 0.f;
#pragma unroll
      for (int m = 0; m < 5; ++m) s += lo_f(xs[m]) + hi_f(xs[m]);
      const float mean = wave_sum(s) * (1.f / D);
      STAMP(18);
      float q2 = 0.f;
#pragma unroll
      for (int m = 0; m < 5; ++m) { const float d0 = lo_f(xs[m]) - mean, d1 = hi_f(xs[m]) - mean; q2 += d0 * d0 + d1 * d1; }
      const float var = rbf(wave_sum(q2) * (1.f / D));
      const float mb = rbf(mean), inv = __builtin_amdgcn_rcpf(rbf(__builtin_amdgcn_sqrtf(rbf(var + EPS))));
#pragma unroll
      for (int e = 0; e < 24; ++e) acc[e] = 0.f;
#pragma unroll
      for (int g = 0; g < 3; ++g) {
        const u32 xl = pk_bf16(rbf(lo_f(xr[g]) - mb) * inv, 0.f), xh = xl << 16;
#pragma unroll
        for (int i = 0; i < 3; ++i)
#pragma unroll
          for (int e = 0; e < 4; ++e) {
            acc[i * 8 + 2 * e] = dot2(wqv[g][i][e], xl, acc[i * 8 + 2 * e]);
            acc[i * 8 + 2 * e + 1] = dot2(wqv[g][i][e], xh, acc[i * 8 + 2 * e + 1]);
          }
      }
      flush24(acc, lp<float>(L, L_PART + w * 768), lane);
    }
    STAMP(1);
    bar();
    u32x4 wov[8];
    {
      const rsrc_t ro = mk(a.wo, H * DH * D * 2);
#pragma unroll
      for (int n = 0; n < 8; ++n) {
        const u32 c = (u32)(480 * w + 64 * n + lane), row = (c * 6554u) >> 16;
        const u32 oo = ((u32)(8 * tj + xg) * DH + row) * (D * 2) + 160u * c8 + (c - 10u * row) * 16u;
        wov[n] = __builtin_amdgcn_raw_buffer_load_b128(ro, (n < 7 || lane < 32) ? oo : OOB, 0, AUX_NT);
      }
    }
    SB();
    make_tag();
    bar();
    if (PADQ) pad(PADQ);
    STAMP(2);
    u32 nxt = (u32)rt() * 16u;
#define PACE_STEP() do { u32 now_ = (u32)rt() * 16u; \
      while ((int)(nxt - now_) > 0) { __builtin_amdgcn_s_sleep(1); now_ = (u32)rt() * 16u; } \
      const u32 lo_ = now_ - PACE_BURST * 16u; if ((int)(lo_ - nxt) > 0) nxt = lo_; nxt += PACE; } while (0)
    LAS u32x4* sp = lp<u32x4>(L, L_S + w * 2048) + r;
    const float SCALE = 0.07216878364870322f;
    u32x4 q[3];
    auto k_block = [&](int bk, bool first) {
      u32x4 sc;
#pragma unroll
      for (int st = 0; st < 4; ++st) {
        PACE_STEP();
        float d[2] = {0.f, 0.f};
#pragma unroll
        for (int i = 0; i < 3; ++i) {
          const int u = 3 * st + i, slot = u % RING;
#pragma unroll
          for (int e = 0; e < 4; ++e) { d[0] = dot2(ring[slot][0][e], q[i][e], d[0]); d[1] = dot2(ring[slot][1][e], q[i][e], d[1]); }
          asm volatile("" : "+v"(d[0]), "+v"(d[1]));
          SB();
          issue(slot, 4 * bk + (u + RING) / 3, (u + RING) % 3);
        }
#pragma unroll
        for (int g = 0; g < 2; ++g) { d[g] += dpp<X1>(d[g]); d[g] += dpp<X2>(d[g]); d[g] += dpp<X7>(d[g]); }
        const u32 s1 = pk_bf16(d[0], d[1]);
        sc[st] = pk_bf16(lo_f(s1) * SCALE, hi_f(s1) * SCALE);
        if (first && st == 1) {
#pragma unroll
          for (int n = 0; n < 8; ++n) if (n < 7 || lane < 32) lp<u32x4>(L, L_WO)[480 * w + 64 * n + lane] = wov[n];
        }
      }
      if (j8 == 0) sp[bk * 8] = sc;
    };
    auto load_q = [&](int t) {
#pragma unroll
      for (int i = 0; i < 3; ++i) q[i] = lp<u32x4>(L, L_Q + t * 384)[j8 + 8 * i];
    };
    load_q(0);
    k_block(0, true);
#pragma unroll 1
    for (int bk = 1; bk < 16; ++bk) {
      if ((bk & 3) == 0) { signal_add((LAS u32*)FL + F_K + (bk >> 2) - 1, lane); wait_flag<1>(FL + F_Q, 1u, dead); load_q(bk >> 2); }
      k_block(bk, false);
    }
    signal_add((LAS u32*)FL + F_K + 3, lane);
    STAMP(3);
#pragma unroll 1
    for (int bk = 0; bk < 16; ++bk) {
      if ((bk & 3) == 0) {
        wait_flag<1>(FL + F_P + (bk >> 2), 1u, dead);
#pragma unroll
        for (int e = 0; e < 24; ++e) acc[e] = 0.f;
      }
      const u32x4 pp = sp[bk * 8];
      if (bk == CPB) FL[F_BLK] = 1u;
#pragma unroll
      for (int st = 0; st < 4; ++st) {
        if (3 * st + RING < 12 || bk < 15) PACE_STEP();
#pragma unroll
        for (int i = 0; i < 3; ++i) {
          const int u = 3 * st + i, slot = u % RING;
#pragma unroll
          for (int e = 0; e < 4; ++e) {
            acc[i * 8 + 2 * e] = dot2(zip_lo(ring[slot][0][e], ring[slot][1][e]), pp[st], acc[i * 8 + 2 * e]);
            acc[i * 8 + 2 * e + 1] = dot2(zip_hi(ring[slot][0][e], ring[slot][1][e]), pp[st], acc[i * 8 + 2 * e + 1]);
          }
#pragma unroll
          for (int e = 0; e < 8; ++e) asm volatile("" : "+v"(acc[i * 8 + e]));
          SB();
          issue(slot, 64 + 4 * bk + (u + RING) / 3, (u + RING) % 3);
        }
      }
      if ((bk & 3) == 3) {
        if (PADV && bk == 15) pad(PADV);
        if (bk == 15) STAMP(4); else STAMP(14 + (bk >> 2));
        flush24(acc, lp<float>(L, L_PART + ((bk >> 2) * 4 + w) * 768), lane);
        if (bk < 15) signal_add((LAS u32*)FL + F_V + (bk >> 2), lane);
      }
    }
    {
      asm volatile("s_waitcnt lgkmcnt(0)" ::: "memory");
      u32 old = 0u;
      if (lane == 0) old = __hip_atomic_fetch_add((LAS u32*)FL + F_L, 1u, __ATOMIC_RELAXED, __HIP_MEMORY_SCOPE_WORKGROUP);
      if (__builtin_amdgcn_readfirstlane(old) == 3u) {
        asm volatile("" ::: "memory");
        publish192(ws, lp<f32x2>(L, L_PART + 3 * 3072), WS_X2 + (u32)(((3 * 8 + xg) * 32 + j) * 96) * 16u, lane, T0, T1);
        STAMP(6);
      }
      SB();
    }
    {
      const u32 og = r < 5 ? (u32)(20 * j + 10 * (w >> 1) + r) * (F * 2u) + 384u * xg + (u32)j8 * 16u : OOB;
      const u32 od = (lane & 15) < 12 ? (u32)(48 * j + 12 * w + (lane >> 4)) * (D * 2u) + ((160u * xg) & ~63u) + (u32)(lane & 15) * 16u : OOB;
      u32 v[9], keep = 0u;
#pragma unroll
      for (int n = 0; n < 6; ++n) v[n] = __builtin_amdgcn_raw_buffer_load_b32(mk((w & 1) ? a.wu : a.wg, D * F * 2), og + (n % 3) * 128, (n / 3) * 5 * (F * 2), 0);
#pragma unroll
      for (int n = 0; n < 3; ++n) v[6 + n] = __builtin_amdgcn_raw_buffer_load_b32(mk(a.wd, F * D * 2), od, n * 4 * (D * 2), 0);
      SB();
#pragma unroll
      for (int n = 0; n < 9; ++n) keep |= v[n];
      asm volatile("" :: "v"(keep));
    }
    __builtin_amdgcn_s_waitcnt(0x0F70);
    STAMP(5);
  }

  if (tj == 3) {
    u32x4 wp[6];
    load_wo(L, w, lane, wp);
#pragma unroll
    for (int e = 0; e < 6; ++e) asm volatile("" : "+v"(wp[e]));
    if (w != SVC) {
      __builtin_amdgcn_s_sleep(PRE);
      { u32 off[1] = {WS_X2 + (u32)(((3 * 8 + xg) * 32 + 8 * w + j8) * 96 + 95) * 16u}, d0[1], d1[1];
        get_recs<1, 2>(ws, off, T0, T1, d0, d1, dead); }
      gather_o<1, SLP_A>(ws, L, 3, xg, w, lane, T0, T1);
    }
    STAMP(7);
    bar();
    f32x2 y = lp<f32x2>(L, L_Y)[8 * w + j8];
    finish_o(L, lp<u32>(L, L_OB + w * 384), lane);
    y += gemv16(lp<u32>(L, L_OB + w * 384), wp, lane);
    put_rec(ws, WS_X3 + (u32)r * X3_REP + (u32)(xg * 320 + 40 * c8 + 8 * w + j8) * 16u, __float_as_uint(y.x), __float_as_uint(y.y), T0, T1);
    SB();
    STAMP(8);
    __builtin_amdgcn_s_barrier();
    SB();
    if (w < 3) {
      u32x4 da[8]; u32 db[8];
      const rsrc_t rd = mk(a.wd, F * D * 2);
      const u32 od = (u32)tid * (8u * D * 2u) + 160u * xg + 20u * c8;
#pragma unroll
      for (int n = 0; n < 8; ++n) {
        da[n] = __builtin_amdgcn_raw_buffer_load_b128(rd, od, n * D * 2, 0);
        db[n] = __builtin_amdgcn_raw_buffer_load_b32(rd, od + 16u, n * D * 2, 0);
      }
      u32 dp[4][10];
#pragma unroll
      for (int n = 0; n < 4; ++n) {
#pragma unroll
        for (int e = 0; e < 4; ++e) { dp[n][2 * e] = zip_lo(da[2 * n][e], da[2 * n + 1][e]); dp[n][2 * e + 1] = zip_hi(da[2 * n][e], da[2 * n + 1][e]); }
        dp[n][8] = zip_lo(db[2 * n], db[2 * n + 1]); dp[n][9] = zip_hi(db[2 * n], db[2 * n + 1]);
#pragma unroll
        for (int e = 0; e < 10; ++e) asm volatile("" : "+v"(dp[n][e]));
      }
      STAMP(10);
      u32 off[2] = {WS_X4 + (u32)xg * X4_REP + (u32)tid * 128u, WS_X4 + (u32)xg * X4_REP + (u32)tid * 128u + 16u}, d0[2], d1[2];
      get_recs<2, SLP_C>(ws, off, T0, T1, d0, d1, dead);
      STAMP(11);
      float c[10], c5[5];
#pragma unroll
      for (int e = 0; e < 10; ++e) c[e] = dot2(dp[3][e], d1[1], dot2(dp[2][e], d0[1], dot2(dp[1][e], d1[0], dot2(dp[0][e], d0[0], 0.f))));
#pragma unroll
      for (int e = 0; e < 5; ++e) c5[e] = tsum32(c[e], c[e + 5]);
      const float v0 = row_sum(tsum16(c5[0], c5[1])), v1 = row_sum(tsum16(c5[2], c5[3])), v2 = row_sum(tsum16(c5[4], c5[4]));
      if ((lane & 15) == 0) {
        LAS float* dn = lp<float>(L, L_DN) + w * 16 + 5 * (lane >> 5);
        const int b4 = (lane >> 4) & 1;
        dn[b4] = v0; dn[2 + b4] = v1; dn[4] = v2;
      }
    } else if (w == 3) {
      const int cp = lane & 7;
      u32 off[1] = {WS_X3 + (u32)xg * X3_REP + (u32)(r * 320 + 40 * xg + 5 * c8 + (cp < 5 ? cp : 0)) * 16u}, d0[1], d1[1];
      const u32 xv = __builtin_amdgcn_raw_buffer_load_b32(rx, (40 * xg + 5 * c8 + (cp < 5 ? cp : 0)) * 4, 0, 0);
      __builtin_amdgcn_s_sleep(PRE);
      get_recs<1, 8>(ws, off, T0, T1, d0, d1, dead);
      float s0 = __uint_as_float(d0[0]), s1 = __uint_as_float(d1[0]);
      s0 = tsum32(s0, s0); s1 = tsum32(s1, s1); s0 = tsum16(s0, s0); s1 = tsum16(s1, s1); s0 += dpp<R8>(s0); s1 += dpp<R8>(s1);
      const u32 at = pk_bf16(s0, s1);
      if (lane < 5) lp<u32>(L, L_H)[lane] = pk_bf16(lo_f(xv) + lo_f(at), hi_f(xv) + hi_f(at));
    }
    bar();
    if (tid < 10) {
      const float s = rbf((lp<float>(L, L_DN)[tid] + lp<float>(L, L_DN)[16 + tid]) + lp<float>(L, L_DN)[32 + tid]);
      a.out[80 * xg + 10 * c8 + tid] = (u16)pk_bf16(lo_f(lp<u16>(L, L_H)[tid]) + s, 0.f);
    }
  } else {
    bar();
    u32 gp[16];
    {
      const rsrc_t rg = mk(a.wg, D * F * 2), ru = mk(a.wu, D * F * 2);
      const u32 og = (u32)tid * (2u * F * 2u) + 384u * xg + 16u * j;
      const u32x4 A = __builtin_amdgcn_raw_buffer_load_b128(rg, og, 0, 0), B = __builtin_amdgcn_raw_buffer_load_b128(rg, og, F * 2, 0);
      const u32x4 C = __builtin_amdgcn_raw_buffer_load_b128(ru, og, 0, 0), E = __builtin_amdgcn_raw_buffer_load_b128(ru, og, F * 2, 0);
#pragma unroll
      for (int e = 0; e < 4; ++e) {
        gp[2 * e] = zip_lo(A[e], B[e]); gp[2 * e + 1] = zip_hi(A[e], B[e]);
        gp[8 + 2 * e] = zip_lo(C[e], E[e]); gp[8 + 2 * e + 1] = zip_hi(C[e], E[e]);
      }
#pragma unroll
      for (int e = 0; e < 16; ++e) asm volatile("" : "+v"(gp[e]));
    }
    STAMP(8);
    if (w == SVC) {
      u32 off[1] = {WS_X3 + (u32)xg * X3_REP + (u32)(r * 320 + 40 * j8 + 39) * 16u}, d0[1], d1[1];
      get_recs<1, 4>(ws, off, T0, T1, d0, d1, dead);
    }
    bar();
    u32 hp;
    {
      u32 off[8], d0[8], d1[8];
#pragma unroll
      for (int k = 0; k < 8; ++k) off[k] = WS_X3 + (u32)xg * X3_REP + (u32)(k * 320 + tid) * 16u;
      get_recs<8, SLP_B>(ws, off, T0, T1, d0, d1, dead);
      auto sum8 = [](const u32 (&d)[8]) {
        float t[4];
#pragma unroll
        for (int k = 0; k < 4; ++k) t[k] = __uint_as_float(d[k]) + __uint_as_float(d[k + 4]);
        return (t[0] + t[2]) + (t[1] + t[3]);
      };
      const u32 at = pk_bf16(sum8(d0), sum8(d1));
      hp = pk_bf16(lo_f(xme) + lo_f(at), hi_f(xme) + hi_f(at));
    }
    STAMP(9);
    const float h0 = lo_f(hp), h1 = hi_f(hp);
    {
      const float mw = wave_sum(h0 + h1) * (1.f / 128.f);
      const float m2 = wave_sum((h0 - mw) * (h0 - mw) + (h1 - mw) * (h1 - mw));
      if (lane == 0) lp<f32x2>(L, L_ST)[w] = f32x2{mw, m2};
    }
    bar();
    if (b == 0 && tid == 0) {
      const u64 e1 = (((u64)ev.y << 32) | ev.x) + 1ull;
      __builtin_amdgcn_raw_buffer_store_b64(u32x2{(u32)e1, (u32)(e1 >> 32)}, ws, WS_E, 0, AUX_SC1);
    }
    f32x2 st[5];
#pragma unroll
    for (int ww = 0; ww < 5; ++ww) st[ww] = lp<f32x2>(L, L_ST)[ww];
    const float mean = (((st[0].x + st[1].x) + (st[2].x + st[3].x)) + st[4].x) * 0.2f;
    float m2 = 0.f;
#pragma unroll
    for (int ww = 0; ww < 5; ++ww) m2 += st[ww].y + 128.f * (st[ww].x - mean) * (st[ww].x - mean);
    const float var = rbf(m2 * (1.f / D));
    const float mb = rbf(mean), inv = __builtin_amdgcn_rcpf(rbf(__builtin_amdgcn_sqrtf(rbf(var + EPS))));
    const u32 dd = pk_bf16(h0 - mb, h1 - mb);
    const u32 hn = pk_bf16(lo_f(dd) * inv, hi_f(dd) * inv);
    float g[16];
#pragma unroll
    for (int e = 0; e < 16; ++e) g[e] = dot2(gp[e], hn, 0.f);
    float g8[8], g4[4], g2[2];
#pragma unroll
    for (int e = 0; e < 8; ++e) g8[e] = tsum32(g[e], g[e + 8]);
#pragma unroll
    for (int e = 0; e < 4; ++e) g4[e] = tsum16(g8[e], g8[e + 4]);
    const bool b3 = (lane & 8) != 0, b2 = (lane & 4) != 0;
#pragma unroll
    for (int e = 0; e < 2; ++e) g2[e] = (b3 ? g4[e + 2] : g4[e]) + dpp<R8>(b3 ? g4[e] : g4[e + 2]);
    float g1 = (b2 ? g2[1] : g2[0]) + dpp<X7>(b2 ? g2[0] : g2[1]);
    g1 += dpp<X2>(g1); g1 += dpp<X1>(g1);
    if ((lane & 3) == 0) lp<float>(L, L_GU)[w * 16 + (lane >> 2)] = g1;
    bar();
    if (w == 0) {
      float gs = 0.f, us = 0.f;
#pragma unroll
      for (int ww = 0; ww < 5; ++ww) { gs += lp<float>(L, L_GU)[ww * 16 + j8]; us += lp<float>(L, L_GU)[ww * 16 + 8 + j8]; }
      const u32 gu = pk_bf16(gs, us);
      const float sg = rbf(__builtin_amdgcn_rcpf(1.f + __builtin_amdgcn_exp2f(-1.4426950408889634f * lo_f(gu))));
      const float f = sg * hi_f(gu);
      const u32 d0 = pk_bf16(dpp<0x00>(f), dpp<0x55>(f)), d1 = pk_bf16(dpp<0xAA>(f), dpp<0xFF>(f));
      put_rec(ws, (lane & 3) == 0 ? WS_X4 + (u32)r * X4_REP + (u32)(xg * 24 + j) * 128u + (u32)(lane & 4) * 4u : OOB, d0, d1, T0, T1);
    }
    STAMP(10);
  }
  if (b == 0 && w == 0) {
    u32 off[4], d0[4], d1[4];
    for (int k = 0; k < 4; ++k) off[k] = WS_XT + (u32)(lane * 4 + k) * 128u;
    get_recs<4, 0>(ws, off, T0, T1, d0, d1, dead);
    float mx = -1e9f, mn = -1e9f;
    for (int k = 0; k < 4; ++k) { const float d = (float)(int)(d0[k] - tstart); mx = fmaxf(mx, d); mn = fmaxf(mn, -d); }
    mx = wave_max(mx); mn = wave_max(mn);
    const int imx = __builtin_amdgcn_readfirstlane((int)mx), imn = __builtin_amdgcn_readfirstlane((int)mn);
    pad(14000 - imn + SKM * (imx + imn));
  }
  STAMP(12);
  asm volatile(".Lfb_end:");
}
}

extern "C" void kernel_launch(void* const* d_in, const int* in_sizes, int n_in, void* d_out, int out_size, void* d_ws, size_t ws_size,
                              hipStream_t stream) {
  Args a;
  a.x = (const u16*)d_in[0]; a.k = (const u16*)d_in[1]; a.v = (const u16*)d_in[2]; a.wq = (const u16*)d_in[3]; a.wo = (const u16*)d_in[4];
  a.wg = (const u16*)d_in[6]; a.wu = (const u16*)d_in[7]; a.wd = (const u16*)d_in[8];
  a.out = (u16*)d_out; a.ws = (unsigned char*)d_ws;
  hipLaunchKernelGGL(fused_block, dim3(256), dim3(NTHR), 0, stream, a);
}
```

```cpp
#include <hip/hip_runtime.h>
#include <hip/hip_bf16.h>
#include <stdint.h>

#define STAMPS 0
#define PACE 1341
#define RING 6
#define PFT 1
#define BG 2
#define SENT_A 0
#define SENT_B 2
#define SLP_A 0
#define SLP_B 0
#define SLP_C 2
#define NOPF 0
#define DUTY0 72
#define DUTY1 40
#define DUTY2 64
#define PFWAIT 1
#define CPF 3
#define CPB 14
#define TCH 3
#define PADQ 0
#define PADV 0
#define PADE 0
#define WOB 1
#define SLP_H 2
#define ANCH 560
#define PDIV 1
#define PF2 1
#define STAG 1341
#define STW 1
#define CONV 0
#define ANCR 0
#define MING 1200
#define GDEL 0
#define GJ 12
#define QDELAY 32
#define PRE 12
#define PACE_BURST 5000

typedef unsigned u32;
typedef unsigned long long u64;
typedef unsigned short u16;
typedef u32 u32x2 __attribute__((ext_vector_type(2)));
typedef u32 u32x3 __attribute__((ext_vector_type(3)));
typedef u32 u32x4 __attribute__((ext_vector_type(4)));
typedef float f32x2 __attribute__((ext_vector_type(2)));
typedef float f32x4 __attribute__((ext_vector_type(4)));
typedef __bf16 bf16x2 __attribute__((ext_vector_type(2)));
typedef __amdgpu_buffer_rsrc_t rsrc_t;
#define LAS __attribute__((address_space(3)))

namespace {
constexpr int D = 640, H = 32, SK = 32768, DH = 192, F = 1536;
constexpr u32 HEADB = (u32)SK * DH * 2;
constexpr int NTHR = 320, SVC = 4;
constexpr u32 OOB = 0x80000000u;
constexpr int AUX_NT = 2, AUX_SC1 = 16;
constexpr u32 WS_E = 0, WS_X1 = 4096, WS_X2 = WS_X1 + 4 * 8 * 32 * 128, WS_X0 = WS_X2 + 4 * 8 * 32 * 1536, WS_XC = WS_X0 + 8 * 32 * 1536,
              WS_XO = WS_XC + 8 * 24 * 640, WS_XT = WS_XO + 8 * 24 * 128, WS_XH = WS_XT + 8 * 8 * 128, WS_X3 = WS_XH + 8 * 64 * 128, X3_REP = 8 * 320 * 16, WS_X4 = WS_X3 + 8 * X3_REP, X4_REP = 192 * 128, WS_BYTES = WS_X4 + 8 * X4_REP;
constexpr u32 WS_STAMP = 4u << 20;
static_assert(WS_BYTES <= WS_STAMP && WS_XC % 128 == 0 && WS_X3 % 128 == 0 && WS_X4 % 128 == 0, "ws");
constexpr int L_Q = 0, L_Y = 1536, L_FLAG = 2048, L_ST = 2176, L_GU = 2240, L_DN = 2560, L_H = 2752, L_OB = 3072, L_RED = 5120,
              L_S = 8192, L_PART = 16384, L_WO = 28672, L_WP = 59392, LDS_BYTES = L_WP + 30720;
constexpr int F_K = 0, F_P = 4, F_V = 8, F_O = 12, F_DEAD = 13, F_Q = 14, F_L = 15, F_BLK = 16, F_END = 17, F_ANC = 18;
constexpr u32 SPIN_G = 1u << 17, SPIN_L = 1u << 21;

struct Args {
  const u16 *x, *k, *v, *wq, *wo, *wg, *wu, *wd;
  u16* out;
  unsigned char* ws;
};

__device__ __forceinline__ float dot2(u32 a, u32 b, float c) {
  return __builtin_amdgcn_fdot2_f32_bf16(__builtin_bit_cast(bf16x2, a), __builtin_bit_cast(bf16x2, b), c, false);
}
__device__ __forceinline__ u32 pk_bf16(float lo, float hi) {
  bf16x2 r = {(__bf16)lo, (__bf16)hi};
  return __builtin_bit_cast(u32, r);
}
__device__ __forceinline__ float lo_f(u32 a) { return __uint_as_float(a << 16); }
__device__ __forceinline__ float hi_f(u32 a) { return __uint_as_float(a & 0xffff0000u); }
__device__ __forceinline__ float rbf(float a) { return lo_f(pk_bf16(a, a)); }
__device__ __forceinline__ u32 zip_lo(u32 a, u32 b) { return __builtin_amdgcn_perm(b, a, 0x05040100u); }
__device__ __forceinline__ u32 zip_hi(u32 a, u32 b) { return __builtin_amdgcn_perm(b, a, 0x07060302u); }
template <int CTRL> __device__ __forceinline__ float dpp(float x) {
  return __builtin_bit_cast(float, __builtin_amdgcn_mov_dpp(__builtin_bit_cast(int, x), CTRL, 0xf, 0xf, true));
}
constexpr int X1 = 0xB1, X2 = 0x4E, X7 = 0x141, R1 = 0x121, R2 = 0x122, R4 = 0x124, R8 = 0x128;
__device__ __forceinline__ float tsum32(float a, float b) {
  auto s = __builtin_amdgcn_permlane32_swap(__float_as_uint(a), __float_as_uint(b), false, false);
  return __uint_as_float(s[0]) + __uint_as_float(s[1]);
}
__device__ __forceinline__ float tsum16(float a, float b) {
  auto s = __builtin_amdgcn_permlane16_swap(__float_as_uint(a), __float_as_uint(b), false, false);
  return __uint_as_float(s[0]) + __uint_as_float(s[1]);
}
__device__ __forceinline__ float row_sum(float x) { x += dpp<R8>(x); x += dpp<X7>(x); x += dpp<X2>(x); x += dpp<X1>(x); return x; }
__device__ __forceinline__ float row_max(float x) {
  x = fmaxf(x, dpp<R8>(x)); x = fmaxf(x, dpp<X7>(x)); x = fmaxf(x, dpp<X2>(x)); x = fmaxf(x, dpp<X1>(x)); return x;
}
__device__ __forceinline__ float wave_sum(float x) { x = row_sum(x); x = tsum16(x, x); return tsum32(x, x); }
__device__ __forceinline__ float wave_max(float x) {
  x = row_max(x);
  auto s = __builtin_amdgcn_permlane16_swap(__float_as_uint(x), __float_as_uint(x), false, false);
  x = fmaxf(__uint_as_float(s[0]), __uint_as_float(s[1]));
  auto t = __builtin_amdgcn_permlane32_swap(__float_as_uint(x), __float_as_uint(x), false, false);
  return fmaxf(__uint_as_float(t[0]), __uint_as_float(t[1]));
}
__device__ __forceinline__ rsrc_t mk(const void* p, u32 bytes) { return __builtin_amdgcn_make_buffer_rsrc((void*)p, 0, bytes, 0x00020000); }
__device__ __forceinline__ void bar() {
  asm volatile("s_waitcnt lgkmcnt(0)" ::: "memory");
  __builtin_amdgcn_s_barrier();
  asm volatile("" ::: "memory");
}
__device__ __forceinline__ void touch2(const void* p, const void* q) {
  u32 d0, d1;
  asm volatile("s_load_dword %0, %2, 0x0\n\ts_load_dword %1, %3, 0x0\n\ts_waitcnt lgkmcnt(0)" : "=&s"(d0), "=&s"(d1) : "s"(p), "s"(q) : "memory");
}
__device__ __forceinline__ u64 rt() { return __builtin_amdgcn_s_memrealtime(); }
#define SB() __builtin_amdgcn_sched_barrier(0)

template <typename T> __device__ __forceinline__ LAS T* lp(LAS unsigned char* l, int off) { return (LAS T*)(l + off); }

__device__ __forceinline__ void put_rec(rsrc_t ws, u32 off, u32 d0, u32 d1, u32 T0, u32 T1) {
  __builtin_amdgcn_raw_buffer_store_b128(u32x4{d0, d1, d0 ^ T0, d1 ^ T1}, ws, off, 0, AUX_SC1);
}
template <int N, int SLEEP>
__device__ __forceinline__ void get_recs(rsrc_t ws, const u32 (&off)[N], u32 T0, u32 T1, u32 (&d0)[N], u32 (&d1)[N], volatile LAS u32* dead,
                                         bool bar_behind_first = false) {
#pragma unroll 1
  for (u32 spin = 0;; ++spin) {
    asm volatile("" ::: "memory");
    u32x4 t[N];
#pragma unroll
    for (int k = 0; k < N; ++k) t[k] = __builtin_amdgcn_raw_buffer_load_b128(ws, off[k], 0, AUX_SC1);
    SB();
    if (bar_behind_first && spin == 0) __builtin_amdgcn_s_barrier();
    u32 bad = 0u;
#pragma unroll
    for (int k = 0; k < N; ++k) { bad |= (t[k].x ^ t[k].z ^ T0) | (t[k].y ^ t[k].w ^ T1); d0[k] = t[k].x; d1[k] = t[k].y; }
    if (!__builtin_amdgcn_ballot_w64(bad != 0u)) break;
    if (SLEEP) __builtin_amdgcn_s_sleep(SLEEP);
    if ((spin & 255u) == 255u) { if (*dead) break; if (spin > SPIN_G) { *dead = 1u; break; } }
  }
}
template <int SLEEP>
__device__ __forceinline__ void wait_flag(volatile LAS u32* f, u32 want, volatile LAS u32* dead) {
  for (u32 spin = 0; (u32)__builtin_amdgcn_readfirstlane(*f) < want; ++spin) {
    if (SLEEP) __builtin_amdgcn_s_sleep(SLEEP);
    if ((spin & 255u) == 255u) { if (*dead) break; if (spin > SPIN_L) { *dead = 1u; break; } }
  }
  asm volatile("" ::: "memory");
}
__device__ __forceinline__ void signal_add(LAS u32* f, int lane) {
  asm volatile("s_waitcnt lgkmcnt(0)" ::: "memory");
  if (lane == 0) __hip_atomic_fetch_add(f, 1u, __ATOMIC_RELAXED, __HIP_MEMORY_SCOPE_WORKGROUP);
}

#define STAMP(i) do { } while (0)

template <int G, int SLEEP>
__device__ __forceinline__ void gather8(rsrc_t ws, LAS unsigned char* L, u32 first, int lane, u32 T0, u32 T1, float (&s)[G][3], bool bbf = false) {
  u32 off[12 * G], d0[12 * G], d1[12 * G];
  const u32 base = first + (u32)((lane >> 5) * 96 + (lane & 31)) * 16u;
#pragma unroll
  for (int n = 0; n < 4 * G; ++n)
#pragma unroll
    for (int pr = 0; pr < 3; ++pr) off[n * 3 + pr] = base + (u32)(2 * n * 96 + 32 * pr) * 16u;
  get_recs<12 * G, SLEEP>(ws, off, T0, T1, d0, d1, lp<u32>(L, L_FLAG) + F_DEAD, bbf);
#pragma unroll
  for (int g = 0; g < G; ++g)
#pragma unroll
    for (int pr = 0; pr < 3; ++pr) {
      const int k = 12 * g + pr;
      float a = (__uint_as_float(d0[k]) + __uint_as_float(d0[k + 3])) + (__uint_as_float(d0[k + 6]) + __uint_as_float(d0[k + 9]));
      float b = (__uint_as_float(d1[k]) + __uint_as_float(d1[k + 3])) + (__uint_as_float(d1[k + 6]) + __uint_as_float(d1[k + 9]));
      s[g][pr] = tsum32(a, b);
    }
}
template <int G, int SLEEP>
__device__ __forceinline__ void gather_o(rsrc_t ws, LAS unsigned char* L, int t, int xg, int vw, int lane, u32 T0, u32 T1) {
  float s[G][3];
  gather8<G, SLEEP>(ws, L, WS_X2 + (u32)(((t * 8 + xg) * 32 + 8 * vw) * 96) * 16u, lane, T0, T1, s);
#pragma unroll
  for (int g = 0; g < G; ++g)
#pragma unroll
    for (int pr = 0; pr < 3; ++pr) lp<float>(L, L_RED)[(((vw + g) * 3 + pr) * 32 + (lane & 31)) * 2 + (lane >> 5)] = s[g][pr];
}
__device__ __forceinline__ void flush24(const float (&acc)[24], LAS float* dst, int lane) {
#pragma unroll
  for (int i = 0; i < 3; ++i) {
    float s1[4], s2[2];
#pragma unroll
    for (int e = 0; e < 4; ++e) s1[e] = tsum32(acc[i * 8 + e], acc[i * 8 + e + 4]);
#pragma unroll
    for (int e = 0; e < 2; ++e) s2[e] = tsum16(s1[e], s1[e + 2]);
    const bool b3 = (lane & 8) != 0;
    const float keep = b3 ? s2[1] : s2[0], send = b3 ? s2[0] : s2[1];
    dst[64 * i + 8 * (lane & 7) + (lane >> 3)] = keep + dpp<R8>(send);
  }
}
__device__ __forceinline__ void publish192(rsrc_t ws, const LAS f32x2* src, u32 first, int lane, u32 T0, u32 T1) {
  const LAS f32x2* p0 = src + lane; const LAS f32x2* p1 = src + 64 + (lane & 31);
  const f32x2 s0 = (p0[0] + p0[96]) + (p0[192] + p0[288]), s1 = (p1[0] + p1[96]) + (p1[192] + p1[288]);
  put_rec(ws, first + (u32)lane * 16u, __float_as_uint(s0.x), __float_as_uint(s0.y), T0, T1);
  put_rec(ws, lane < 32 ? first + 1024u + (u32)lane * 16u : OOB, __float_as_uint(s1.x), __float_as_uint(s1.y), T0, T1);
}
__device__ __forceinline__ void pack_wo(LAS unsigned char* L, int vw, int lane) {
  const LAS u32* src = lp<u32>(L, L_WO) + (lane >> 3) * 960 + 8 * vw + (lane & 7);
#pragma unroll
  for (int m = 0; m < 12; m += 2) {
    const u32 A = src[80 * m], B = src[80 * m + 40], C = src[80 * m + 80], E = src[80 * m + 120];
    lp<u32x4>(L, L_WP)[(vw * 6 + m / 2) * 64 + lane] = u32x4{zip_lo(A, B), zip_hi(A, B), zip_lo(C, E), zip_hi(C, E)};
  }
}
__device__ __forceinline__ void load_wo(LAS unsigned char* L, int vw, int lane, u32x4 (&wp)[6]) {
#pragma unroll
  for (int m = 0; m < 6; ++m) wp[m] = lp<u32x4>(L, L_WP)[(vw * 6 + m) * 64 + lane];
}
__device__ __forceinline__ void finish_o(LAS unsigned char* L, LAS u32* ob, int lane) {
  const LAS f32x4* rp = lp<f32x4>(L, L_RED) + (lane < 48 ? lane : 0);
  const f32x4 s = (rp[0] + rp[48]) + (rp[96] + rp[144]);
  if (lane < 48) ((LAS u32x2*)ob)[lane] = u32x2{pk_bf16(s.x, s.y), pk_bf16(s.z, s.w)};
  asm volatile("s_waitcnt lgkmcnt(0)" ::: "memory");
}
__device__ __forceinline__ f32x2 gemv16(const LAS u32* ob, const u32x4 (&wp)[6], int lane) {
  const LAS u32x4* op = (const LAS u32x4*)ob + (lane >> 3) * 3;
  const u32x4 o0 = op[0], o1 = op[1], o2 = op[2];
  float y[4] = {0.f, 0.f, 0.f, 0.f};
#pragma unroll
  for (int m = 0; m < 12; ++m) {
    const u32 o = m < 4 ? o0[m & 3] : m < 8 ? o1[m & 3] : o2[m & 3];
    y[2 * (m & 1)] = dot2(wp[m >> 1][2 * (m & 1)], o, y[2 * (m & 1)]); y[2 * (m & 1) + 1] = dot2(wp[m >> 1][2 * (m & 1) + 1], o, y[2 * (m & 1) + 1]);
  }
  float y0 = y[0] + y[2], y1 = y[1] + y[3];
  y0 += dpp<R8>(y0); y1 += dpp<R8>(y1);
  y0 = tsum16(y0, y0); y1 = tsum16(y1, y1);
  return f32x2{tsum32(y0, y0), tsum32(y1, y1)};
}

__global__ void __launch_bounds__(NTHR, 1) fused_block(Args a) {
  __shared__ __attribute__((aligned(16))) unsigned char lds_raw[LDS_BYTES];
  LAS unsigned char* L = (LAS unsigned char*)lds_raw;
  const int tid = threadIdx.x, lane = tid & 63, w = __builtin_amdgcn_readfirstlane(tid >> 6);
  const int b = blockIdx.x, xg = b & 7, j = b >> 3;
  const int r = lane >> 3, j8 = lane & 7;
  const int tj = j >> 3, c8 = j & 7;
  volatile LAS u32* FL = lp<u32>(L, L_FLAG);
  volatile LAS u32* dead = FL + F_DEAD;
  STAMP(0);
  const u32 tstart = (u32)rt();
  auto pad = [&](u32 ticks) { while ((u32)rt() - tstart < ticks) __builtin_amdgcn_s_sleep(1); };
  const rsrc_t ws = mk(a.ws, WS_BYTES);
  const rsrc_t rx = mk(a.x, D * 2), rq = mk(a.wq, D * H * DH * 2);

  const float EPS = 1e-5f;
  u64 pc0; u32 clen;
  asm volatile("s_getpc_b64 %0\n.Lfb_begin:\n\ts_mov_b32 %1, .Lfb_end-.Lfb_begin" : "=s"(pc0), "=s"(clen));
  auto code_touch = [&](int piece) {
    const u32 v = __builtin_amdgcn_raw_buffer_load_b32(mk((const void*)pc0, clen), (u32)piece * 1024u + (u32)lane * 16u, 0, 0);
    asm volatile("" :: "v"(v));
  };
  u32 eoff = WS_E; asm volatile("" : "+v"(eoff));
  const u32 voff = (u32)r * 384u + (u32)j8 * 16u;
  const u32 sbase = (u32)xg * HEADB + (u32)j * 24576u + (u32)w * 6144u;
  auto step_off = [&](int n) -> u32 { return sbase + (u32)(n >> 4) * (8u * HEADB) + (u32)(n & 15) * (32u * 24576u); };
  u32x2 ev; u32 xme, T0, T1;
  auto make_tag = [&]() {
    u64 z = (((u64)ev.y << 32) | ev.x) + 0x9E3779B97F4A7C15ull;
    z = (z ^ (z >> 30)) * 0xBF58476D1CE4E5B9ull; z = (z ^ (z >> 27)) * 0x94D049BB133111EBull; z ^= z >> 31;
    T0 = __builtin_amdgcn_readfirstlane((u32)z) | 1u; T1 = __builtin_amdgcn_readfirstlane((u32)(z >> 32)) | 1u;
  };

  if (w == SVC) {
    ev = __builtin_amdgcn_raw_buffer_load_b64(ws, eoff, 0, AUX_SC1);
    xme = __builtin_amdgcn_raw_buffer_load_b32(rx, tid * 4, 0, 0);
    if (lane < 32) FL[lane] = 0u;
    if (CPF & 1) code_touch(j);
    if ((TCH & 1) && j >= 26) { const char* p = (const char*)a.k + (size_t)xg * HEADB + (size_t)(j - 25) * 2097152 - 64; touch2(p, p); }
    make_tag();
    STAMP(1);
    bar();
    publish192(ws, lp<f32x2>(L, L_PART), WS_X0 + (u32)((xg * 32 + j) * 96) * 16u, lane, T0, T1);
    if (ANCR) { const u32 now = (u32)rt(); put_rec(ws, (lane == 0 && j < 8) ? WS_XT + (u32)(xg * 8 + j) * 128u : OOB, now, now, T0, T1); }
    __builtin_amdgcn_s_sleep(QDELAY);
    u32x4 tv;
    if (ANCR) tv = __builtin_amdgcn_raw_buffer_load_b128(ws, WS_XT + (u32)(xg * 8 + j8) * 128u, 0, AUX_SC1);
#pragma unroll 1
    for (int t = 0; t < 4; ++t) {
      float s[1][3];
      gather8<1, 4>(ws, L, WS_X0 + (u32)((xg * 32 + 8 * t) * 96) * 16u, lane, T0, T1, s, WOB && t == 0);
#pragma unroll
      for (int pr = 0; pr < 3; ++pr) lp<u16>(L, L_Q)[t * 192 + (32 * pr + (lane & 31)) * 2 + (lane >> 5)] = (u16)pk_bf16(s[0][pr], 0.f);
      if (t == 0) {
        if (ANCR) {
          const bool ok = ((tv.x ^ tv.z ^ T0) | (tv.y ^ tv.w ^ T1)) == 0u;
          float m = ok ? (float)(int)(tv.x - tstart) : 1e9f;
          m = fmaxf(m, dpp<X1>(m)); m = fmaxf(m, dpp<X2>(m)); m = fmaxf(m, dpp<X7>(m));
          FL[F_ANC] = m < 2000.f ? tstart + (u32)(int)m + ANCR : (u32)rt();
        }
        STAMP(2); bar();
        if (TCH & 2) {
          auto at = [&](int n) -> const char* {
            if (n < 49) { const int sl = n / 7 + 1, pt = n % 7;
              return (const char*)(sl < 4 ? a.k : a.v) + (size_t)(8 * (sl & 3) + xg) * HEADB + (pt ? (size_t)pt * 2097152 - 64 : 0); }
            const int m = n - 49, e = (m & 1) * (D * F * 2 - 64);
            return m < 2 ? (const char*)a.wg + e : m < 4 ? (const char*)a.wu + e : m < 6 ? (const char*)a.wd + e : m < 8 ? (const char*)a.out : (const char*)a.ws + (m & 1) * (WS_BYTES - 64);
          };
          touch2(at(j), at(j + 32));
        }
      }
      __builtin_amdgcn_s_sleep(64);
    }
    signal_add((LAS u32*)FL + F_Q, lane);
#pragma unroll 1
    for (int t = 0; t < 4; ++t) {
      wait_flag<8>(FL + F_K + t, 4u, dead);
      if (t == 0) {
#pragma unroll 1
        for (int vw = 0; vw < 5; ++vw) pack_wo(L, vw, lane);
      }
      LAS u32x4* sp = lp<u32x4>(L, L_S + ((lane >> 4) * 4 + t) * 512 + (lane & 15) * 32);
      const u32x4 s0 = sp[0], s1 = sp[1];
      float s[16];
#pragma unroll
      for (int e = 0; e < 4; ++e) { s[2 * e] = lo_f(s0[e]); s[2 * e + 1] = hi_f(s0[e]); s[8 + 2 * e] = lo_f(s1[e]); s[8 + 2 * e + 1] = hi_f(s1[e]); }
      float m = s[0];
#pragma unroll
      for (int e = 1; e < 16; ++e) m = fmaxf(m, s[e]);
      m = wave_max(m);
      const float L2E = 1.4426950408889634f;
      float l = 0.f;
#pragma unroll
      for (int e = 0; e < 16; ++e) l += __builtin_amdgcn_exp2f((s[e] - m) * L2E);
      l = wave_sum(l);
      put_rec(ws, lane == 0 ? WS_X1 + (u32)((t * 8 + xg) * 32 + j) * 128u : OOB, __float_as_uint(m), __float_as_uint(l), T0, T1);
      u32 off[1] = {WS_X1 + (u32)((t * 8 + xg) * 32 + (lane & 31)) * 128u}, d0[1], d1[1];
      __builtin_amdgcn_s_sleep(32);
      get_recs<1, 32>(ws, off, T0, T1, d0, d1, dead);
      const float mj = __uint_as_float(d0[0]), lj = __uint_as_float(d1[0]);
      const float M = wave_max(mj);
      float Ls = row_sum(lj * __builtin_amdgcn_exp2f((mj - M) * L2E));
      Ls = tsum16(Ls, Ls);
      Ls = __uint_as_float(__builtin_amdgcn_readfirstlane(__float_as_uint(Ls)));
      const float inv = 1.f / Ls;
      u32x4 p0, p1;
#pragma unroll
      for (int e = 0; e < 4; ++e) {
        p0[e] = pk_bf16(__builtin_amdgcn_exp2f((s[2 * e] - M) * L2E) * inv, __builtin_amdgcn_exp2f((s[2 * e + 1] - M) * L2E) * inv);
        p1[e] = pk_bf16(__builtin_amdgcn_exp2f((s[8 + 2 * e] - M) * L2E) * inv, __builtin_amdgcn_exp2f((s[8 + 2 * e + 1] - M) * L2E) * inv);
      }
      sp[0] = p0; sp[1] = p1;
      signal_add((LAS u32*)FL + F_P + t, lane);
    }
#pragma unroll 1
    for (int t = 0; t < 3; ++t) {
      wait_flag<8>(FL + F_V + t, 4u, dead);
      if (t == 2) STAMP(3);
      publish192(ws, lp<f32x2>(L, L_PART + t * 3072), WS_X2 + (u32)(((t * 8 + xg) * 32 + j) * 96) * 16u, lane, T0, T1);
      if (tj == t) {
        __builtin_amdgcn_s_sleep(DUTY0);
        {
          const int rc = lane & 15;
          u32 off[8], d0[8], d1[8];
#pragma unroll
          for (int n = 0; n < 8; ++n) off[n] = WS_X2 + (u32)(((t * 8 + xg) * 32 + 4 * n + (lane >> 4)) * 96 + 12 * c8 + (rc < 12 ? rc : 0)) * 16u;
          get_recs<8, 8>(ws, off, T0, T1, d0, d1, dead);
          if (t == 2) STAMP(4);
          float s0 = 0.f, s1 = 0.f;
#pragma unroll
          for (int n = 0; n < 8; ++n) { s0 += __uint_as_float(d0[n]); s1 += __uint_as_float(d1[n]); }
          s0 = tsum16(s0, s0); s1 = tsum16(s1, s1); s0 = tsum32(s0, s0); s1 = tsum32(s1, s1);
          const u32 op = pk_bf16(s0, s1), on = __float_as_uint(dpp<X1>(__uint_as_float(op)));
          put_rec(ws, (lane < 12 && !(lane & 1)) ? WS_XO + (u32)(xg * 24 + j) * 128u + (u32)lane * 8u : OOB, op, on, T0, T1);
        }
        __builtin_amdgcn_s_sleep(DUTY1);
        {
          const int l6 = (lane * 43) >> 8;
          u32 off[1] = {WS_XO + (lane < 48 ? (u32)(xg * 24 + 8 * t + l6) * 128u + (u32)(lane - 6 * l6) * 16u : (u32)(xg * 24 + 8 * t) * 128u)}, d0[1], d1[1];
          get_recs<1, 8>(ws, off, T0, T1, d0, d1, dead);
          if (lane < 48) lp<u32x2>(L, L_OB)[lane] = u32x2{d0[0], d1[0]};
          asm volatile("s_waitcnt lgkmcnt(0)" ::: "memory");
        }
        if (t == 2) STAMP(5);
#pragma unroll 1
        for (int vw = 0; vw < 5; ++vw) {
          u32x4 wp[6];
          load_wo(L, vw, lane, wp);
          f32x2 y = gemv16(lp<u32>(L, L_OB), wp, lane);
          if (t > 0) y += lp<f32x2>(L, L_Y)[8 * vw + j8];
          put_rec(ws, lane < 8 ? WS_XC + (u32)(xg * 24 + j) * 640u + (u32)(8 * vw + lane) * 16u : OOB, __float_as_uint(y.x), __float_as_uint(y.y), T0, T1);
        }
      }
      if (tj == t + 1) {
        __builtin_amdgcn_s_sleep(127); __builtin_amdgcn_s_sleep(127); __builtin_amdgcn_s_sleep(DUTY2);
        u32 off[1] = {WS_XC + (u32)(xg * 24 + j - 8) * 640u + (u32)(lane < 40 ? lane : 0) * 16u}, d0[1], d1[1];
        get_recs<1, 8>(ws, off, T0, T1, d0, d1, dead);
        if (lane < 40) lp<u32x2>(L, L_Y)[lane] = u32x2{d0[0], d1[0]};
      }
      if (t == 2) STAMP(13);
    }
    if ((CPF & 2) && tj < 2) { wait_flag<8>(FL + F_BLK, 1u, dead); code_touch(2 * j); code_touch(2 * j + 1); }
    if (PF2 && tj < 2) {
      const u32 og = (u32)(40 * j + r) * (F * 2u) + 384u * xg + (u32)j8 * 16u;
      const u32 od = (lane & 15) < 12 ? (u32)(96 * j + (lane >> 4)) * (D * 2u) + ((160u * xg) & ~63u) + (u32)(lane & 15) * 16u : OOB;
      const rsrc_t rg = mk(a.wg, D * F * 2), ru = mk(a.wu, D * F * 2), rd = mk(a.wd, F * D * 2);
      u32 v[54], keep = 0u;
      wait_flag<1>(FL + F_END, 4u, dead);
#pragma unroll
      for (int n = 0; n < 30; ++n) v[n] = __builtin_amdgcn_raw_buffer_load_b32((n & 1) ? ru : rg, og + ((n >> 1) % 3) * 128, (n / 6) * 8 * (F * 2), 0);
#pragma unroll
      for (int n = 0; n < 24; ++n) v[30 + n] = __builtin_amdgcn_raw_buffer_load_b32(rd, od, n * 4 * (D * 2), 0);
      SB();
#pragma unroll
      for (int n = 0; n < 54; ++n) keep |= v[n];
      asm volatile("" :: "v"(keep));
      STAMP(6);
    }
  } else {
    u32 xs[5]; u16 xr[3]; u32x4 wqv[3][3]; u32x4 ring[RING][2];
#pragma unroll
    for (int m = 0; m < 5; ++m) xs[m] = __builtin_amdgcn_raw_buffer_load_b32(rx, (lane + 64 * m) * 4, 0, 0);
    SB();
    {
      const u32 row = (u32)(80 * (j & 7) + 20 * w + r);
      const u32 o = row * (u32)(H * DH * 2) + (u32)(8 * (j >> 3) + xg) * 384u + (u32)j8 * 16u;
#pragma unroll
      for (int g = 0; g < 3; ++g) {
        const u32 og = (g < 2 || r < 4) ? o : OOB, ox = (g < 2 || r < 4) ? row * 2u : OOB;
#pragma unroll
        for (int i = 0; i < 3; ++i) wqv[g][i] = __builtin_amdgcn_raw_buffer_load_b128(rq, og + i * 128, g * 8 * (H * DH * 2), AUX_NT);
        xr[g] = __builtin_amdgcn_raw_buffer_load_b16(rx, ox, g * 16, 0);
      }
    }
    ev = __builtin_amdgcn_raw_buffer_load_b64(ws, eoff, 0, AUX_SC1);
    xme = __builtin_amdgcn_raw_buffer_load_b32(rx, tid * 4, 0, 0);
    SB();
    auto issue = [&](int slot, int n, int i) {
      const rsrc_t rs = mk(n < 64 ? a.k : a.v, n < 128 ? HEADB * H : 0u);
      const u32 so = step_off(n & 63);
      ring[slot][0] = __builtin_amdgcn_raw_buffer_load_b128(rs, voff + i * 128, so, AUX_NT);
      ring[slot][1] = __builtin_amdgcn_raw_buffer_load_b128(rs, voff + 3072 + i * 128, so, AUX_NT);
      SB();
    };
#pragma unroll
    for (int u = 0; u < RING; ++u) issue(u, u / 3, u % 3);
    float acc[24];
    {
      float s = 0.f;
#pragma unroll
      for (int m = 0; m < 5; ++m) s += lo_f(xs[m]) + hi_f(xs[m]);
      const float mean = wave_sum(s) * (1.f / D);
      STAMP(18);
      float q2 = 0.f;
#pragma unroll
      for (int m = 0; m < 5; ++m) { const float d0 = lo_f(xs[m]) - mean, d1 = hi_f(xs[m]) - mean; q2 += d0 * d0 + d1 * d1; }
      const float var = rbf(wave_sum(q2) * (1.f / D));
      const float mb = rbf(mean), inv = __builtin_amdgcn_rcpf(rbf(__builtin_amdgcn_sqrtf(rbf(var + EPS))));
#pragma unroll
      for (int e = 0; e < 24; ++e) acc[e] = 0.f;
#pragma unroll
      for (int g = 0; g < 3; ++g) {
        const u32 xl = pk_bf16(rbf(lo_f(xr[g]) - mb) * inv, 0.f), xh = xl << 16;
#pragma unroll
        for (int i = 0; i < 3; ++i)
#pragma unroll
          for (int e = 0; e < 4; ++e) {
            acc[i * 8 + 2 * e] = dot2(wqv[g][i][e], xl, acc[i * 8 + 2 * e]);
            acc[i * 8 + 2 * e + 1] = dot2(wqv[g][i][e], xh, acc[i * 8 + 2 * e + 1]);
          }
      }
      flush24(acc, lp<float>(L, L_PART + w * 768), lane);
    }
    STAMP(1);
    bar();
    make_tag();
    __builtin_amdgcn_s_barrier();
    u32x4 wov[8];
    {
      const rsrc_t ro = mk(a.wo, H * DH * D * 2);
#pragma unroll
      for (int n = 0; n < 8; ++n) {
        const u32 c = (u32)(480 * w + 64 * n + lane), row = (c * 6554u) >> 16;
        const u32 oo = ((u32)(8 * tj + xg) * DH + row) * (D * 2) + 160u * c8 + (c - 10u * row) * 16u;
        wov[n] = __builtin_amdgcn_raw_buffer_load_b128(ro, (n < 7 || lane < 32) ? oo : OOB, 0, AUX_NT);
      }
    }
    SB();
    bar();
    if (PADQ) pad(PADQ);
    STAMP(2);
    u32 nxt = (ANCR ? (u32)__builtin_amdgcn_readfirstlane(FL[F_ANC]) : ANCH ? tstart + ANCH : (u32)rt()) * 16u + (u32)((j * 4 + w * STW) * STAG) / 128u;
    u32 sch = nxt;
    const u32 per = PACE / PDIV - (u32)(j * CONV) / 32u;
#define PACE_STEP() do { u32 now_ = (u32)rt() * 16u; \
      while ((int)(nxt - now_) > 0) { __builtin_amdgcn_s_sleep(1); now_ = (u32)rt() * 16u; } \
      sch += per; nxt = now_ + MING / PDIV; if ((int)(sch - nxt) > 0) nxt = sch; } while (0)
    LAS u32x4* sp = lp<u32x4>(L, L_S + w * 2048) + r;
    const float SCALE = 0.07216878364870322f;
    u32x4 q[3];
    auto k_block = [&](int bk, bool first) {
      u32x4 sc;
#pragma unroll
      for (int st = 0; st < 4; ++st) {
        float d[2] = {0.f, 0.f};
#pragma unroll
        for (int i = 0; i < 3; ++i) {
          const int u = 3 * st + i, slot = u % RING;
          if (i == 0 || PDIV == 3) PACE_STEP();
#pragma unroll
          for (int e = 0; e < 4; ++e) { d[0] = dot2(ring[slot][0][e], q[i][e], d[0]); d[1] = dot2(ring[slot][1][e], q[i][e], d[1]); }
          asm volatile("" : "+v"(d[0]), "+v"(d[1]));
          SB();
          issue(slot, 4 * bk + (u + RING) / 3, (u + RING) % 3);
        }
#pragma unroll
        for (int g = 0; g < 2; ++g) { d[g] += dpp<X1>(d[g]); d[g] += dpp<X2>(d[g]); d[g] += dpp<X7>(d[g]); }
        const u32 s1 = pk_bf16(d[0], d[1]);
        sc[st] = pk_bf16(lo_f(s1) * SCALE, hi_f(s1) * SCALE);
        if (first && st == 1) {
#pragma unroll
          for (int n = 0; n < 8; ++n) if (n < 7 || lane < 32) lp<u32x4>(L, L_WO)[480 * w + 64 * n + lane] = wov[n];
        }
      }
      if (j8 == 0) sp[bk * 8] = sc;
    };
    auto load_q = [&](int t) {
#pragma unroll
      for (int i = 0; i < 3; ++i) q[i] = lp<u32x4>(L, L_Q + t * 384)[j8 + 8 * i];
    };
    load_q(0);
    k_block(0, true);
#pragma unroll 1
    for (int bk = 1; bk < 16; ++bk) {
      if ((bk & 3) == 0) { signal_add((LAS u32*)FL + F_K + (bk >> 2) - 1, lane); wait_flag<1>(FL + F_Q, 1u, dead); load_q(bk >> 2); }
      k_block(bk, false);
    }
    signal_add((LAS u32*)FL + F_K + 3, lane);
    STAMP(3);
#pragma unroll 1
    for (int bk = 0; bk < 16; ++bk) {
      if ((bk & 3) == 0) {
        wait_flag<1>(FL + F_P + (bk >> 2), 1u, dead);
#pragma unroll
        for (int e = 0; e < 24; ++e) acc[e] = 0.f;
      }
      const u32x4 pp = sp[bk * 8];
      if (bk == CPB) FL[F_BLK] = 1u;
#pragma unroll
      for (int st = 0; st < 4; ++st) {
#pragma unroll
        for (int i = 0; i < 3; ++i) {
          const int u = 3 * st + i, slot = u % RING;
          if ((i == 0 || PDIV == 3) && (u + RING < 12 || bk < 15)) PACE_STEP();
#pragma unroll
          for (int e = 0; e < 4; ++e) {
            acc[i * 8 + 2 * e] = dot2(zip_lo(ring[slot][0][e], ring[slot][1][e]), pp[st], acc[i * 8 + 2 * e]);
            acc[i * 8 + 2 * e + 1] = dot2(zip_hi(ring[slot][0][e], ring[slot][1][e]), pp[st], acc[i * 8 + 2 * e + 1]);
          }
#pragma unroll
          for (int e = 0; e < 8; ++e) asm volatile("" : "+v"(acc[i * 8 + e]));
          SB();
          issue(slot, 64 + 4 * bk + (u + RING) / 3, (u + RING) % 3);
          if (u + RING == 11 && bk == 15 && lane == 0) __hip_atomic_fetch_add((LAS u32*)FL + F_END, 1u, __ATOMIC_RELAXED, __HIP_MEMORY_SCOPE_WORKGROUP);
        }
      }
      if ((bk & 3) == 3) {
        if (PADV && bk == 15) pad(PADV);
        if (bk == 15) STAMP(4); else STAMP(14 + (bk >> 2));
        flush24(acc, lp<float>(L, L_PART + ((bk >> 2) * 4 + w) * 768), lane);
        if (bk < 15) signal_add((LAS u32*)FL + F_V + (bk >> 2), lane);
      }
    }
    auto publish_last = [&]() {
      asm volatile("s_waitcnt lgkmcnt(0)" ::: "memory");
      u32 old = 0u;
      if (lane == 0) old = __hip_atomic_fetch_add((LAS u32*)FL + F_L, 1u, __ATOMIC_RELAXED, __HIP_MEMORY_SCOPE_WORKGROUP);
      if (__builtin_amdgcn_readfirstlane(old) == 3u) {
        asm volatile("" ::: "memory");
        publish192(ws, lp<f32x2>(L, L_PART + 3 * 3072), WS_X2 + (u32)(((3 * 8 + xg) * 32 + j) * 96) * 16u, lane, T0, T1);
        STAMP(6);
      }
      SB();
    };
    auto ring_done = [&]() {
#pragma unroll
      for (int u = 0; u < RING; ++u) asm volatile("" :: "v"(ring[u][0]), "v"(ring[u][1]));
      SB();
    };
    if (tj == 3) { ring_done(); publish_last(); } else { publish_last(); ring_done(); }
    STAMP(5);
  }

  if (tj == 3) {
    u32x4 wp[6];
    load_wo(L, w, lane, wp);
#pragma unroll
    for (int e = 0; e < 6; ++e) asm volatile("" : "+v"(wp[e]));
    if (w != SVC) {
      __builtin_amdgcn_s_sleep(PRE);
      gather_o<1, SLP_A>(ws, L, 3, xg, w, lane, T0, T1);
    }
    STAMP(7);
    bar();
    if (w == SVC) put_rec(ws, lane < 8 ? WS_XH + (u32)(lane * 64 + xg * 8 + c8) * 128u : OOB, 0u, 0u, T0, T1);
    f32x2 y = lp<f32x2>(L, L_Y)[8 * w + j8];
    finish_o(L, lp<u32>(L, L_OB + w * 384), lane);
    y += gemv16(lp<u32>(L, L_OB + w * 384), wp, lane);
    put_rec(ws, WS_X3 + (u32)r * X3_REP + (u32)(xg * 320 + 40 * c8 + 8 * w + j8) * 16u, __float_as_uint(y.x), __float_as_uint(y.y), T0, T1);
    SB();
    STAMP(8);
    __builtin_amdgcn_s_barrier();
    SB();
    if (w < 3) {
      u32x4 da[8]; u32 db[8];
      const rsrc_t rd = mk(a.wd, F * D * 2);
      const u32 od = (u32)tid * (8u * D * 2u) + 160u * xg + 20u * c8;
#pragma unroll
      for (int n = 0; n < 8; ++n) {
        da[n] = __builtin_amdgcn_raw_buffer_load_b128(rd, od, n * D * 2, 0);
        db[n] = __builtin_amdgcn_raw_buffer_load_b32(rd, od + 16u, n * D * 2, 0);
      }
      u32 dp[4][10];
#pragma unroll
      for (int n = 0; n < 4; ++n) {
#pragma unroll
        for (int e = 0; e < 4; ++e) { dp[n][2 * e] = zip_lo(da[2 * n][e], da[2 * n + 1][e]); dp[n][2 * e + 1] = zip_hi(da[2 * n][e], da[2 * n + 1][e]); }
        dp[n][8] = zip_lo(db[2 * n], db[2 * n + 1]); dp[n][9] = zip_hi(db[2 * n], db[2 * n + 1]);
#pragma unroll
        for (int e = 0; e < 10; ++e) asm volatile("" : "+v"(dp[n][e]));
      }
      STAMP(10);
      u32 off[2] = {WS_X4 + (u32)xg * X4_REP + (u32)tid * 128u, WS_X4 + (u32)xg * X4_REP + (u32)tid * 128u + 16u}, d0[2], d1[2];
      get_recs<2, SLP_C>(ws, off, T0, T1, d0, d1, dead);
      STAMP(11);
      float c[10], c5[5];
#pragma unroll
      for (int e = 0; e < 10; ++e) c[e] = dot2(dp[3][e], d1[1], dot2(dp[2][e], d0[1], dot2(dp[1][e], d1[0], dot2(dp[0][e], d0[0], 0.f))));
#pragma unroll
      for (int e = 0; e < 5; ++e) c5[e] = tsum32(c[e], c[e + 5]);
      const float v0 = row_sum(tsum16(c5[0], c5[1])), v1 = row_sum(tsum16(c5[2], c5[3])), v2 = row_sum(tsum16(c5[4], c5[4]));
      if ((lane & 15) == 0) {
        LAS float* dn = lp<float>(L, L_DN) + w * 16 + 5 * (lane >> 5);
        const int b4 = (lane >> 4) & 1;
        dn[b4] = v0; dn[2 + b4] = v1; dn[4] = v2;
      }
    } else if (w == 3) {
      const int cp = lane & 7;
      u32 off[1] = {WS_X3 + (u32)xg * X3_REP + (u32)(r * 320 + 40 * xg + 5 * c8 + (cp < 5 ? cp : 0)) * 16u}, d0[1], d1[1];
      const u32 xv = __builtin_amdgcn_raw_buffer_load_b32(rx, (40 * xg + 5 * c8 + (cp < 5 ? cp : 0)) * 4, 0, 0);
      __builtin_amdgcn_s_sleep(PRE);
      get_recs<1, 8>(ws, off, T0, T1, d0, d1, dead);
      float s0 = __uint_as_float(d0[0]), s1 = __uint_as_float(d1[0]);
      s0 = tsum32(s0, s0); s1 = tsum32(s1, s1); s0 = tsum16(s0, s0); s1 = tsum16(s1, s1); s0 += dpp<R8>(s0); s1 += dpp<R8>(s1);
      const u32 at = pk_bf16(s0, s1);
      if (lane < 5) lp<u32>(L, L_H)[lane] = pk_bf16(lo_f(xv) + lo_f(at), hi_f(xv) + hi_f(at));
    }
    bar();
    if (tid < 10) {
      const float s = rbf((lp<float>(L, L_DN)[tid] + lp<float>(L, L_DN)[16 + tid]) + lp<float>(L, L_DN)[32 + tid]);
      a.out[80 * xg + 10 * c8 + tid] = (u16)pk_bf16(lo_f(lp<u16>(L, L_H)[tid]) + s, 0.f);
    }
  } else {
    bar();
    if (GDEL && j >= GJ) __builtin_amdgcn_s_sleep(GDEL);
    u32 gp[16];
    {
      u32 pf[4];
      const u32 od = ((lane & 15) < 12 && w < 4 && !NOPF && !PF2) ? (u32)(64 * j + 16 * w + (lane >> 4)) * (D * 2u) + ((160u * xg) & ~63u) + (u32)(lane & 15) * 16u : OOB;
#pragma unroll
      for (int n = 0; n < 4; ++n) pf[n] = __builtin_amdgcn_raw_buffer_load_b32(mk(a.wd, F * D * 2), od, n * 4 * (D * 2), 0);
      SB();
      const rsrc_t rg = mk(a.wg, D * F * 2), ru = mk(a.wu, D * F * 2);
      const u32 og = (u32)tid * (2u * F * 2u) + 384u * xg + 16u * j;
      const u32x4 A = __builtin_amdgcn_raw_buffer_load_b128(rg, og, 0, 0), B = __builtin_amdgcn_raw_buffer_load_b128(rg, og, F * 2, 0);
      const u32x4 C = __builtin_amdgcn_raw_buffer_load_b128(ru, og, 0, 0), E = __builtin_amdgcn_raw_buffer_load_b128(ru, og, F * 2, 0);
#pragma unroll
      for (int e = 0; e < 4; ++e) {
        gp[2 * e] = zip_lo(A[e], B[e]); gp[2 * e + 1] = zip_hi(A[e], B[e]);
        gp[8 + 2 * e] = zip_lo(C[e], E[e]); gp[8 + 2 * e + 1] = zip_hi(C[e], E[e]);
      }
#pragma unroll
      for (int e = 0; e < 16; ++e) asm volatile("" : "+v"(gp[e]));
      asm volatile("" :: "v"(pf[0] | pf[1] | pf[2] | pf[3]));
    }
    STAMP(8);
    if (w == SVC) {
      u32 off[1] = {SENT_B == 2 ? WS_XH + (u32)(xg * 64 + lane) * 128u : WS_X3 + (u32)xg * X3_REP + (u32)(r * 320 + 40 * j8 + 39) * 16u}, d0[1], d1[1];
      get_recs<1, SLP_H>(ws, off, T0, T1, d0, d1, dead);
    }
    bar();
    u32 hp;
    {
      u32 off[8], d0[8], d1[8];
#pragma unroll
      for (int k = 0; k < 8; ++k) off[k] = WS_X3 + (u32)xg * X3_REP + (u32)(k * 320 + tid) * 16u;
      get_recs<8, SLP_B>(ws, off, T0, T1, d0, d1, dead);
      auto sum8 = [](const u32 (&d)[8]) {
        float t[4];
#pragma unroll
        for (int k = 0; k < 4; ++k) t[k] = __uint_as_float(d[k]) + __uint_as_float(d[k + 4]);
        return (t[0] + t[2]) + (t[1] + t[3]);
      };
      const u32 at = pk_bf16(sum8(d0), sum8(d1));
      hp = pk_bf16(lo_f(xme) + lo_f(at), hi_f(xme) + hi_f(at));
    }
    STAMP(9);
    const float h0 = lo_f(hp), h1 = hi_f(hp);
    {
      const float c = __uint_as_float(__builtin_amdgcn_readfirstlane(__float_as_uint(h0)));
      const float e0 = h0 - c, e1 = h1 - c;
      const float s = row_sum(tsum16(e0 + e1, e0 * e0 + e1 * e1));
      const float t = tsum32(s, s);
      auto p = __builtin_amdgcn_permlane16_swap(__float_as_uint(t), __float_as_uint(t), false, false);
      const float s1 = __uint_as_float(p[0]) * (1.f / 128.f);
      if (lane == 0) lp<f32x2>(L, L_ST)[w] = f32x2{c + s1, __uint_as_float(p[1]) - 128.f * s1 * s1};
    }
    bar();
    if (b == 0 && tid == 0) {
      const u64 e1 = (((u64)ev.y << 32) | ev.x) + 1ull;
      __builtin_amdgcn_raw_buffer_store_b64(u32x2{(u32)e1, (u32)(e1 >> 32)}, ws, WS_E, 0, AUX_SC1);
    }
    f32x2 st[5];
#pragma unroll
    for (int ww = 0; ww < 5; ++ww) st[ww] = lp<f32x2>(L, L_ST)[ww];
    const float mean = (((st[0].x + st[1].x) + (st[2].x + st[3].x)) + st[4].x) * 0.2f;
    float m2 = 0.f;
#pragma unroll
    for (int ww = 0; ww < 5; ++ww) m2 += st[ww].y + 128.f * (st[ww].x - mean) * (st[ww].x - mean);
    const float var = rbf(m2 * (1.f / D));
    const float mb = rbf(mean), inv = __builtin_amdgcn_rcpf(rbf(__builtin_amdgcn_sqrtf(rbf(var + EPS))));
    const u32 dd = pk_bf16(h0 - mb, h1 - mb);
    const u32 hn = pk_bf16(lo_f(dd) * inv, hi_f(dd) * inv);
    float g[16];
#pragma unroll
    for (int e = 0; e < 16; ++e) g[e] = dot2(gp[e], hn, 0.f);
    float g8[8], g4[4], g2[2];
#pragma unroll
    for (int e = 0; e < 8; ++e) g8[e] = tsum32(g[e], g[e + 8]);
#pragma unroll
    for (int e = 0; e < 4; ++e) g4[e] = tsum16(g8[e], g8[e + 4]);
    const bool b3 = (lane & 8) != 0, b2 = (lane & 4) != 0;
#pragma unroll
    for (int e = 0; e < 2; ++e) g2[e] = (b3 ? g4[e + 2] : g4[e]) + dpp<R8>(b3 ? g4[e] : g4[e + 2]);
    float g1 = (b2 ? g2[1] : g2[0]) + dpp<X7>(b2 ? g2[0] : g2[1]);
    g1 += dpp<X2>(g1); g1 += dpp<X1>(g1);
    if ((lane & 3) == 0) lp<float>(L, L_GU)[w * 16 + (lane >> 2)] = g1;
    bar();
    if (w == 0) {
      float gs = 0.f, us = 0.f;
#pragma unroll
      for (int ww = 0; ww < 5; ++ww) { gs += lp<float>(L, L_GU)[ww * 16 + j8]; us += lp<float>(L, L_GU)[ww * 16 + 8 + j8]; }
      const u32 gu = pk_bf16(gs, us);
      const float sg = rbf(__builtin_amdgcn_rcpf(1.f + __builtin_amdgcn_exp2f(-1.4426950408889634f * lo_f(gu))));
      const float f = sg * hi_f(gu);
      const u32 d0 = pk_bf16(dpp<0x00>(f), dpp<0x55>(f)), d1 = pk_bf16(dpp<0xAA>(f), dpp<0xFF>(f));
      put_rec(ws, (lane & 3) == 0 ? WS_X4 + (u32)r * X4_REP + (u32)(xg * 24 + j) * 128u + (u32)(lane & 4) * 4u : OOB, d0, d1, T0, T1);
    }
    STAMP(10);
  }
  if (PADE) pad(PADE);
  STAMP(12);
  asm volatile(".Lfb_end:");
}
}

extern "C" void kernel_launch(void* const* d_in, const int* in_sizes, int n_in, void* d_out, int out_size, void* d_ws, size_t ws_size,
                              hipStream_t stream) {
  Args a;
  a.x = (const u16*)d_in[0]; a.k = (const u16*)d_in[1]; a.v = (const u16*)d_in[2]; a.wq = (const u16*)d_in[3]; a.wo = (const u16*)d_in[4];
  a.wg = (const u16*)d_in[6]; a.wu = (const u16*)d_in[7]; a.wd = (const u16*)d_in[8];
  a.out = (u16*)d_out; a.ws = (unsigned char*)d_ws;
  hipLaunchKernelGGL(fused_block, dim3(256), dim3(NTHR), 0, stream, a);
}
```

```cpp
#include <hip/hip_runtime.h>
#include <hip/hip_bf16.h>
#include <stdint.h>

#define STAMPS 0
#define PACE 1341
#define RING 6
#define PFT 1
#define BG 2
#define SENT_A 0
#define SENT_B 2
#define SLP_A 0
#define SLP_B 0
#define SLP_C 2
#define NOPF 0
#define DUTY0 72
#define DUTY1 40
#define DUTY2 64
#define PFWAIT 1
#define CPF 3
#define CPB 14
#define TCH 3
#define PADQ 0
#define PADV 0
#define PADE 0
#define WOB 1
#define SLP_H 2
#define ANCH 520
#define PDIV 1
#define PF2 1
#define STAG 1341
#define STW 1
#define CONV 0
#define ANCR 200
#define MING 1200
#define GDEL 0
#define GJ 12
#define DLT 0
#define TH 640
#define WU 1
#define VMS 1
#define VDL 0
#define VTH 64
#define EWS 2
#define LATEOK 0
#define VDN 16
#define VLO 40
#define VSK 0
#define QDELAY 32
#define PRE 12
#define PACE_BURST 5000

typedef unsigned u32;
typedef unsigned long long u64;
typedef unsigned short u16;
typedef u32 u32x2 __attribute__((ext_vector_type(2)));
typedef u32 u32x3 __attribute__((ext_vector_type(3)));
typedef u32 u32x4 __attribute__((ext_vector_type(4)));
typedef float f32x2 __attribute__((ext_vector_type(2)));
typedef float f32x4 __attribute__((ext_vector_type(4)));
typedef __bf16 bf16x2 __attribute__((ext_vector_type(2)));
typedef __amdgpu_buffer_rsrc_t rsrc_t;
#define LAS __attribute__((address_space(3)))

namespace {
constexpr int D = 640, H = 32, SK = 32768, DH = 192, F = 1536;
constexpr u32 HEADB = (u32)SK * DH * 2;
constexpr int NTHR = 320, SVC = 4;
constexpr u32 OOB = 0x80000000u;
constexpr int AUX_NT = 2, AUX_SC1 = 16;
constexpr u32 WS_E = 0, WS_X1 = 4096, WS_X2 = WS_X1 + 4 * 8 * 32 * 128, WS_X0 = WS_X2 + 4 * 8 * 32 * 1536, WS_XC = WS_X0 + 8 * 32 * 1536,
              WS_XO = WS_XC + 8 * 24 * 640, WS_XT = WS_XO + 8 * 24 * 128, WS_XH = WS_XT + 8 * 8 * 128, WS_X3 = WS_XH + 8 * 64 * 128, X3_REP = 8 * 320 * 16, WS_X4 = WS_X3 + 8 * X3_REP, X4_REP = 192 * 128, WS_BYTES = WS_X4 + 8 * X4_REP;
constexpr u32 WS_STAMP = 4u << 20;
static_assert(WS_BYTES <= WS_STAMP && WS_XC % 128 == 0 && WS_X3 % 128 == 0 && WS_X4 % 128 == 0, "ws");
constexpr int L_Q = 0, L_Y = 1536, L_FLAG = 2048, L_ST = 2176, L_GU = 2240, L_DN = 2560, L_H = 2752, L_OB = 3072, L_RED = 5120,
              L_S = 8192, L_PART = 16384, L_WO = 28672, L_WP = 59392, LDS_BYTES = L_WP + 30720;
constexpr int F_K = 0, F_P = 4, F_V = 8, F_O = 12, F_DEAD = 13, F_Q = 14, F_L = 15, F_BLK = 16, F_END = 17, F_ANC = 18;
constexpr u32 SPIN_G = 1u << 17, SPIN_L = 1u << 21;

struct Args {
  const u16 *x, *k, *v, *wq, *wo, *wg, *wu, *wd;
  u16* out;
  unsigned char* ws;
};

__device__ __forceinline__ float dot2(u32 a, u32 b, float c) {
  return __builtin_amdgcn_fdot2_f32_bf16(__builtin_bit_cast(bf16x2, a), __builtin_bit_cast(bf16x2, b), c, false);
}
__device__ __forceinline__ u32 pk_bf16(float lo, float hi) {
  bf16x2 r = {(__bf16)lo, (__bf16)hi};
  return __builtin_bit_cast(u32, r);
}
__device__ __forceinline__ float lo_f(u32 a) { return __uint_as_float(a << 16); }
__device__ __forceinline__ float hi_f(u32 a) { return __uint_as_float(a & 0xffff0000u); }
__device__ __forceinline__ float rbf(float a) { return lo_f(pk_bf16(a, a)); }
__device__ __forceinline__ u32 zip_lo(u32 a, u32 b) { return __builtin_amdgcn_perm(b, a, 0x05040100u); }
__device__ __forceinline__ u32 zip_hi(u32 a, u32 b) { return __builtin_amdgcn_perm(b, a, 0x07060302u); }
template <int CTRL> __device__ __forceinline__ float dpp(float x) {
  return __builtin_bit_cast(float, __builtin_amdgcn_mov_dpp(__builtin_bit_cast(int, x), CTRL, 0xf, 0xf, true));
}
constexpr int X1 = 0xB1, X2 = 0x4E, X7 = 0x141, R1 = 0x121, R2 = 0x122, R4 = 0x124, R8 = 0x128;
__device__ __forceinline__ float tsum32(float a, float b) {
  auto s = __builtin_amdgcn_permlane32_swap(__float_as_uint(a), __float_as_uint(b), false, false);
  return __uint_as_float(s[0]) + __uint_as_float(s[1]);
}
__device__ __forceinline__ float tsum16(float a, float b) {
  auto s = __builtin_amdgcn_permlane16_swap(__float_as_uint(a), __float_as_uint(b), false, false);
  return __uint_as_float(s[0]) + __uint_as_float(s[1]);
}
__device__ __forceinline__ float row_sum(float x) { x += dpp<R8>(x); x += dpp<X7>(x); x += dpp<X2>(x); x += dpp<X1>(x); return x; }
__device__ __forceinline__ float row_max(float x) {
  x = fmaxf(x, dpp<R8>(x)); x = fmaxf(x, dpp<X7>(x)); x = fmaxf(x, dpp<X2>(x)); x = fmaxf(x, dpp<X1>(x)); return x;
}
__device__ __forceinline__ float wave_sum(float x) { x = row_sum(x); x = tsum16(x, x); return tsum32(x, x); }
__device__ __forceinline__ float wave_max(float x) {
  x = row_max(x);
  auto s = __builtin_amdgcn_permlane16_swap(__float_as_uint(x), __float_as_uint(x), false, false);
  x = fmaxf(__uint_as_float(s[0]), __uint_as_float(s[1]));
  auto t = __builtin_amdgcn_permlane32_swap(__float_as_uint(x), __float_as_uint(x), false, false);
  return fmaxf(__uint_as_float(t[0]), __uint_as_float(t[1]));
}
__device__ __forceinline__ rsrc_t mk(const void* p, u32 bytes) { return __builtin_amdgcn_make_buffer_rsrc((void*)p, 0, bytes, 0x00020000); }
__device__ __forceinline__ void bar() {
  asm volatile("s_waitcnt lgkmcnt(0)" ::: "memory");
  __builtin_amdgcn_s_barrier();
  asm volatile("" ::: "memory");
}
__device__ __forceinline__ void touch2(const void* p, const void* q) {
  u32 d0, d1;
  asm volatile("s_load_dword %0, %2, 0x0\n\ts_load_dword %1, %3, 0x0\n\ts_waitcnt lgkmcnt(0)" : "=&s"(d0), "=&s"(d1) : "s"(p), "s"(q) : "memory");
}
__device__ __forceinline__ u64 rt() { return __builtin_amdgcn_s_memrealtime(); }
#define SB() __builtin_amdgcn_sched_barrier(0)

template <typename T> __device__ __forceinline__ LAS T* lp(LAS unsigned char* l, int off) { return (LAS T*)(l + off); }

__device__ __forceinline__ void put_rec(rsrc_t ws, u32 off, u32 d0, u32 d1, u32 T0, u32 T1) {
  __builtin_amdgcn_raw_buffer_store_b128(u32x4{d0, d1, d0 ^ T0, d1 ^ T1}, ws, off, 0, AUX_SC1);
}
template <int N, int SLEEP>
__device__ __forceinline__ void get_recs(rsrc_t ws, const u32 (&off)[N], u32 T0, u32 T1, u32 (&d0)[N], u32 (&d1)[N], volatile LAS u32* dead,
                                         bool bar_behind_first = false) {
#pragma unroll 1
  for (u32 spin = 0;; ++spin) {
    asm volatile("" ::: "memory");
    u32x4 t[N];
#pragma unroll
    for (int k = 0; k < N; ++k) t[k] = __builtin_amdgcn_raw_buffer_load_b128(ws, off[k], 0, AUX_SC1);
    SB();
    if (bar_behind_first && spin == 0) __builtin_amdgcn_s_barrier();
    u32 bad = 0u;
#pragma unroll
    for (int k = 0; k < N; ++k) { bad |= (t[k].x ^ t[k].z ^ T0) | (t[k].y ^ t[k].w ^ T1); d0[k] = t[k].x; d1[k] = t[k].y; }
    if (!__builtin_amdgcn_ballot_w64(bad != 0u)) break;
    if (SLEEP) __builtin_amdgcn_s_sleep(SLEEP);
    if ((spin & 255u) == 255u) { if (*dead) break; if (spin > SPIN_G) { *dead = 1u; break; } }
  }
}
template <int SLEEP>
__device__ __forceinline__ void wait_flag(volatile LAS u32* f, u32 want, volatile LAS u32* dead) {
  for (u32 spin = 0; (u32)__builtin_amdgcn_readfirstlane(*f) < want; ++spin) {
    if (SLEEP) __builtin_amdgcn_s_sleep(SLEEP);
    if ((spin & 255u) == 255u) { if (*dead) break; if (spin > SPIN_L) { *dead = 1u; break; } }
  }
  asm volatile("" ::: "memory");
}
__device__ __forceinline__ void signal_add(LAS u32* f, int lane) {
  asm volatile("s_waitcnt lgkmcnt(0)" ::: "memory");
  if (lane == 0) __hip_atomic_fetch_add(f, 1u, __ATOMIC_RELAXED, __HIP_MEMORY_SCOPE_WORKGROUP);
}

#define STAMP(i) do { } while (0)
#define VREC(i) do { } while (0)

template <int G, int SLEEP>
__device__ __forceinline__ void gather8(rsrc_t ws, LAS unsigned char* L, u32 first, int lane, u32 T0, u32 T1, float (&s)[G][3], bool bbf = false) {
  u32 off[12 * G], d0[12 * G], d1[12 * G];
  const u32 base = first + (u32)((lane >> 5) * 96 + (lane & 31)) * 16u;
#pragma unroll
  for (int n = 0; n < 4 * G; ++n)
#pragma unroll
    for (int pr = 0; pr < 3; ++pr) off[n * 3 + pr] = base + (u32)(2 * n * 96 + 32 * pr) * 16u;
  get_recs<12 * G, SLEEP>(ws, off, T0, T1, d0, d1, lp<u32>(L, L_FLAG) + F_DEAD, bbf);
#pragma unroll
  for (int g = 0; g < G; ++g)
#pragma unroll
    for (int pr = 0; pr < 3; ++pr) {
      const int k = 12 * g + pr;
      float a = (__uint_as_float(d0[k]) + __uint_as_float(d0[k + 3])) + (__uint_as_float(d0[k + 6]) + __uint_as_float(d0[k + 9]));
      float b = (__uint_as_float(d1[k]) + __uint_as_float(d1[k + 3])) + (__uint_as_float(d1[k + 6]) + __uint_as_float(d1[k + 9]));
      s[g][pr] = tsum32(a, b);
    }
}
template <int G, int SLEEP>
__device__ __forceinline__ void gather_o(rsrc_t ws, LAS unsigned char* L, int t, int xg, int vw, int lane, u32 T0, u32 T1) {
  float s[G][3];
  gather8<G, SLEEP>(ws, L, WS_X2 + (u32)(((t * 8 + xg) * 32 + 8 * vw) * 96) * 16u, lane, T0, T1, s);
#pragma unroll
  for (int g = 0; g < G; ++g)
#pragma unroll
    for (int pr = 0; pr < 3; ++pr) lp<float>(L, L_RED)[(((vw + g) * 3 + pr) * 32 + (lane & 31)) * 2 + (lane >> 5)] = s[g][pr];
}
__device__ __forceinline__ void flush24(const float (&acc)[24], LAS float* dst, int lane) {
#pragma unroll
  for (int i = 0; i < 3; ++i) {
    float s1[4], s2[2];
#pragma unroll
    for (int e = 0; e < 4; ++e) s1[e] = tsum32(acc[i * 8 + e], acc[i * 8 + e + 4]);
#pragma unroll
    for (int e = 0; e < 2; ++e) s2[e] = tsum16(s1[e], s1[e + 2]);
    const bool b3 = (lane & 8) != 0;
    const float keep = b3 ? s2[1] : s2[0], send = b3 ? s2[0] : s2[1];
    dst[64 * i + 8 * (lane & 7) + (lane >> 3)] = keep + dpp<R8>(send);
  }
}
__device__ __forceinline__ void publish192(rsrc_t ws, const LAS f32x2* src, u32 first, int lane, u32 T0, u32 T1) {
  const LAS f32x2* p0 = src + lane; const LAS f32x2* p1 = src + 64 + (lane & 31);
  const f32x2 s0 = (p0[0] + p0[96]) + (p0[192] + p0[288]), s1 = (p1[0] + p1[96]) + (p1[192] + p1[288]);
  put_rec(ws, first + (u32)lane * 16u, __float_as_uint(s0.x), __float_as_uint(s0.y), T0, T1);
  put_rec(ws, lane < 32 ? first + 1024u + (u32)lane * 16u : OOB, __float_as_uint(s1.x), __float_as_uint(s1.y), T0, T1);
}
__device__ __forceinline__ void pack_wo(LAS unsigned char* L, int vw, int lane) {
  const LAS u32* src = lp<u32>(L, L_WO) + (lane >> 3) * 960 + 8 * vw + (lane & 7);
#pragma unroll
  for (int m = 0; m < 12; m += 2) {
    const u32 A = src[80 * m], B = src[80 * m + 40], C = src[80 * m + 80], E = src[80 * m + 120];
    lp<u32x4>(L, L_WP)[(vw * 6 + m / 2) * 64 + lane] = u32x4{zip_lo(A, B), zip_hi(A, B), zip_lo(C, E), zip_hi(C, E)};
  }
}
__device__ __forceinline__ void load_wo(LAS unsigned char* L, int vw, int lane, u32x4 (&wp)[6]) {
#pragma unroll
  for (int m = 0; m < 6; ++m) wp[m] = lp<u32x4>(L, L_WP)[(vw * 6 + m) * 64 + lane];
}
__device__ __forceinline__ void finish_o(LAS unsigned char* L, LAS u32* ob, int lane) {
  const LAS f32x4* rp = lp<f32x4>(L, L_RED) + (lane < 48 ? lane : 0);
  const f32x4 s = (rp[0] + rp[48]) + (rp[96] + rp[144]);
  if (lane < 48) ((LAS u32x2*)ob)[lane] = u32x2{pk_bf16(s.x, s.y), pk_bf16(s.z, s.w)};
  asm volatile("s_waitcnt lgkmcnt(0)" ::: "memory");
}
__device__ __forceinline__ f32x2 gemv16(const LAS u32* ob, const u32x4 (&wp)[6], int lane) {
  const LAS u32x4* op = (const LAS u32x4*)ob + (lane >> 3) * 3;
  const u32x4 o0 = op[0], o1 = op[1], o2 = op[2];
  float y[4] = {0.f, 0.f, 0.f, 0.f};
#pragma unroll
  for (int m = 0; m < 12; ++m) {
    const u32 o = m < 4 ? o0[m & 3] : m < 8 ? o1[m & 3] : o2[m & 3];
    y[2 * (m & 1)] = dot2(wp[m >> 1][2 * (m & 1)], o, y[2 * (m & 1)]); y[2 * (m & 1) + 1] = dot2(wp[m >> 1][2 * (m & 1) + 1], o, y[2 * (m & 1) + 1]);
  }
  float y0 = y[0] + y[2], y1 = y[1] + y[3];
  y0 += dpp<R8>(y0); y1 += dpp<R8>(y1);
  y0 = tsum16(y0, y0); y1 = tsum16(y1, y1);
  return f32x2{tsum32(y0, y0), tsum32(y1, y1)};
}

__global__ void __launch_bounds__(NTHR, 1) fused_block(Args a) {
  __shared__ __attribute__((aligned(16))) unsigned char lds_raw[LDS_BYTES];
  LAS unsigned char* L = (LAS unsigned char*)lds_raw;
  const int tid = threadIdx.x, lane = tid & 63, w = __builtin_amdgcn_readfirstlane(tid >> 6);
  const int b = blockIdx.x, xg = b & 7, j = b >> 3;
  const int r = lane >> 3, j8 = lane & 7;
  const int tj = j >> 3, c8 = j & 7;
  volatile LAS u32* FL = lp<u32>(L, L_FLAG);
  volatile LAS u32* dead = FL + F_DEAD;
  STAMP(0);
  const u64 t64 = rt();
  const u32 tstart = (u32)t64;
  auto pad = [&](u32 ticks) { while ((u32)rt() - tstart < ticks) __builtin_amdgcn_s_sleep(1); };
  const rsrc_t ws = mk(a.ws, WS_BYTES);
  const rsrc_t rx = mk(a.x, D * 2), rq = mk(a.wq, D * H * DH * 2);

  const float EPS = 1e-5f;
  u64 pc0; u32 clen;
  asm volatile("s_getpc_b64 %0\n.Lfb_begin:\n\ts_mov_b32 %1, .Lfb_end-.Lfb_begin" : "=s"(pc0), "=s"(clen));
  auto code_touch = [&](int piece) {
    const u32 v = __builtin_amdgcn_raw_buffer_load_b32(mk((const void*)pc0, clen), (u32)piece * 1024u + (u32)lane * 16u, 0, 0);
    asm volatile("" :: "v"(v));
  };
  u32 eoff = WS_E; asm volatile("" : "+v"(eoff));
  const u32 voff = (u32)r * 384u + (u32)j8 * 16u;
  const u32 sbase = (u32)xg * HEADB + (u32)j * 24576u + (u32)w * 6144u;
  auto step_off = [&](int n) -> u32 { return sbase + (u32)(n >> 4) * (8u * HEADB) + (u32)(n & 15) * (32u * 24576u); };
  u32x2 ev; u32 xme, T0, T1;
  auto make_tag = [&]() {
    u64 z = (((u64)ev.y << 32) | ev.x) + 0x9E3779B97F4A7C15ull;
    z = (z ^ (z >> 30)) * 0xBF58476D1CE4E5B9ull; z = (z ^ (z >> 27)) * 0x94D049BB133111EBull; z ^= z >> 31;
    T0 = __builtin_amdgcn_readfirstlane((u32)z) | 1u; T1 = __builtin_amdgcn_readfirstlane((u32)(z >> 32)) | 1u;
  };

  if (w == SVC) {
    ev = __builtin_amdgcn_raw_buffer_load_b64(ws, eoff, 0, AUX_SC1);
    xme = __builtin_amdgcn_raw_buffer_load_b32(rx, tid * 4, 0, 0);
    if (lane < 32) FL[lane] = 0u;
    if (CPF & 1) code_touch(j);
    if ((TCH & 1) && j >= 26) { const char* p = (const char*)a.k + (size_t)xg * HEADB + (size_t)(j - 25) * 2097152 - 64; touch2(p, p); }
    make_tag();
    STAMP(1);
    bar();
    publish192(ws, lp<f32x2>(L, L_PART), WS_X0 + (u32)((xg * 32 + j) * 96) * 16u, lane, T0, T1);
    if (ANCR) { const u32 now = (u32)rt(); put_rec(ws, (lane == 0 && j < 8) ? WS_XT + (u32)(xg * 8 + j) * 128u : OOB, now, now, T0, T1); }
    __builtin_amdgcn_s_sleep(QDELAY);
    u32x4 tv;
    if (ANCR) tv = __builtin_amdgcn_raw_buffer_load_b128(ws, WS_XT + (u32)(xg * 8 + j8) * 128u, 0, AUX_SC1);
#pragma unroll 1
    for (int t = 0; t < 4; ++t) {
      float s[1][3];
      gather8<1, 4>(ws, L, WS_X0 + (u32)((xg * 32 + 8 * t) * 96) * 16u, lane, T0, T1, s, WOB && t == 0);
#pragma unroll
      for (int pr = 0; pr < 3; ++pr) lp<u16>(L, L_Q)[t * 192 + (32 * pr + (lane & 31)) * 2 + (lane >> 5)] = (u16)pk_bf16(s[0][pr], 0.f);
      if (t == 0) {
        if (ANCR) {
          const bool ok = ((tv.x ^ tv.z ^ T0) | (tv.y ^ tv.w ^ T1)) == 0u;
          float m = ok ? (float)(int)(tv.x - tstart) : 1e9f;
          m = fmaxf(m, dpp<X1>(m)); m = fmaxf(m, dpp<X2>(m)); m = fmaxf(m, dpp<X7>(m));
          FL[F_ANC] = m < 2000.f ? tstart + (u32)(int)m + ANCR : (u32)rt();
        }
        STAMP(2); bar();
        if (TCH & 2) {
          auto at = [&](int n) -> const char* {
            if (n < 49) { const int sl = n / 7 + 1, pt = n % 7;
              return (const char*)(sl < 4 ? a.k : a.v) + (size_t)(8 * (sl & 3) + xg) * HEADB + (pt ? (size_t)pt * 2097152 - 64 : 0); }
            const int m = n - 49, e = (m & 1) * (D * F * 2 - 64);
            return m < 2 ? (const char*)a.wg + e : m < 4 ? (const char*)a.wu + e : m < 6 ? (const char*)a.wd + e : m < 8 ? (const char*)a.out : (const char*)a.ws + (m & 1) * (WS_BYTES - 64);
          };
          touch2(at(j), at(j + 32));
        }
      }
      __builtin_amdgcn_s_sleep(64);
    }
    signal_add((LAS u32*)FL + F_Q, lane);
#pragma unroll 1
    for (int t = 0; t < 4; ++t) {
      wait_flag<8>(FL + F_K + t, 4u, dead);
      if (t == 0) {
#pragma unroll 1
        for (int vw = 0; vw < 5; ++vw) pack_wo(L, vw, lane);
      }
      LAS u32x4* sp = lp<u32x4>(L, L_S + ((lane >> 4) * 4 + t) * 512 + (lane & 15) * 32);
      const u32x4 s0 = sp[0], s1 = sp[1];
      float s[16];
#pragma unroll
      for (int e = 0; e < 4; ++e) { s[2 * e] = lo_f(s0[e]); s[2 * e + 1] = hi_f(s0[e]); s[8 + 2 * e] = lo_f(s1[e]); s[8 + 2 * e + 1] = hi_f(s1[e]); }
      float m = s[0];
#pragma unroll
      for (int e = 1; e < 16; ++e) m = fmaxf(m, s[e]);
      m = wave_max(m);
      const float L2E = 1.4426950408889634f;
      float l = 0.f;
#pragma unroll
      for (int e = 0; e < 16; ++e) l += __builtin_amdgcn_exp2f((s[e] - m) * L2E);
      l = wave_sum(l);
      put_rec(ws, lane == 0 ? WS_X1 + (u32)((t * 8 + xg) * 32 + j) * 128u : OOB, __float_as_uint(m), __float_as_uint(l), T0, T1);
      u32 off[1] = {WS_X1 + (u32)((t * 8 + xg) * 32 + (lane & 31)) * 128u}, d0[1], d1[1];
      __builtin_amdgcn_s_sleep(32);
      get_recs<1, 32>(ws, off, T0, T1, d0, d1, dead);
      const float mj = __uint_as_float(d0[0]), lj = __uint_as_float(d1[0]);
      const float M = wave_max(mj);
      float Ls = row_sum(lj * __builtin_amdgcn_exp2f((mj - M) * L2E));
      Ls = tsum16(Ls, Ls);
      Ls = __uint_as_float(__builtin_amdgcn_readfirstlane(__float_as_uint(Ls)));
      const float inv = 1.f / Ls;
      u32x4 p0, p1;
#pragma unroll
      for (int e = 0; e < 4; ++e) {
        p0[e] = pk_bf16(__builtin_amdgcn_exp2f((s[2 * e] - M) * L2E) * inv, __builtin_amdgcn_exp2f((s[2 * e + 1] - M) * L2E) * inv);
        p1[e] = pk_bf16(__builtin_amdgcn_exp2f((s[8 + 2 * e] - M) * L2E) * inv, __builtin_amdgcn_exp2f((s[8 + 2 * e + 1] - M) * L2E) * inv);
      }
      sp[0] = p0; sp[1] = p1;
      signal_add((LAS u32*)FL + F_P + t, lane);
    }
#pragma unroll 1
    for (int t = 0; t < 3; ++t) {
      wait_flag<8>(FL + F_V + t, 4u, dead);
      if (t == 2) STAMP(3);
      publish192(ws, lp<f32x2>(L, L_PART + t * 3072), WS_X2 + (u32)(((t * 8 + xg) * 32 + j) * 96) * 16u, lane, T0, T1);
      if (tj == t) {
        __builtin_amdgcn_s_sleep(DUTY0);
        {
          const int rc = lane & 15;
          u32 off[8], d0[8], d1[8];
#pragma unroll
          for (int n = 0; n < 8; ++n) off[n] = WS_X2 + (u32)(((t * 8 + xg) * 32 + 4 * n + (lane >> 4)) * 96 + 12 * c8 + (rc < 12 ? rc : 0)) * 16u;
          get_recs<8, 8>(ws, off, T0, T1, d0, d1, dead);
          if (t == 2) STAMP(4);
          float s0 = 0.f, s1 = 0.f;
#pragma unroll
          for (int n = 0; n < 8; ++n) { s0 += __uint_as_float(d0[n]); s1 += __uint_as_float(d1[n]); }
          s0 = tsum16(s0, s0); s1 = tsum16(s1, s1); s0 = tsum32(s0, s0); s1 = tsum32(s1, s1);
          const u32 op = pk_bf16(s0, s1), on = __float_as_uint(dpp<X1>(__uint_as_float(op)));
          put_rec(ws, (lane < 12 && !(lane & 1)) ? WS_XO + (u32)(xg * 24 + j) * 128u + (u32)lane * 8u : OOB, op, on, T0, T1);
        }
        __builtin_amdgcn_s_sleep(DUTY1);
        {
          const int l6 = (lane * 43) >> 8;
          u32 off[1] = {WS_XO + (lane < 48 ? (u32)(xg * 24 + 8 * t + l6) * 128u + (u32)(lane - 6 * l6) * 16u : (u32)(xg * 24 + 8 * t) * 128u)}, d0[1], d1[1];
          get_recs<1, 8>(ws, off, T0, T1, d0, d1, dead);
          if (lane < 48) lp<u32x2>(L, L_OB)[lane] = u32x2{d0[0], d1[0]};
          asm volatile("s_waitcnt lgkmcnt(0)" ::: "memory");
        }
        if (t == 2) STAMP(5);
#pragma unroll 1
        for (int vw = 0; vw < 5; ++vw) {
          u32x4 wp[6];
          load_wo(L, vw, lane, wp);
          f32x2 y = gemv16(lp<u32>(L, L_OB), wp, lane);
          if (t > 0) y += lp<f32x2>(L, L_Y)[8 * vw + j8];
          put_rec(ws, lane < 8 ? WS_XC + (u32)(xg * 24 + j) * 640u + (u32)(8 * vw + lane) * 16u : OOB, __float_as_uint(y.x), __float_as_uint(y.y), T0, T1);
        }
      }
      if (tj == t + 1) {
        __builtin_amdgcn_s_sleep(127); __builtin_amdgcn_s_sleep(127); __builtin_amdgcn_s_sleep(DUTY2);
        u32 off[1] = {WS_XC + (u32)(xg * 24 + j - 8) * 640u + (u32)(lane < 40 ? lane : 0) * 16u}, d0[1], d1[1];
        get_recs<1, 8>(ws, off, T0, T1, d0, d1, dead);
        if (lane < 40) lp<u32x2>(L, L_Y)[lane] = u32x2{d0[0], d1[0]};
      }
      if (t == 2) STAMP(13);
    }
    if ((CPF & 2) && tj < 2) { wait_flag<8>(FL + F_BLK, 1u, dead); code_touch(2 * j); code_touch(2 * j + 1); }
    if (PF2 && tj < 2) {
      const u32 og = (u32)(40 * j + r) * (F * 2u) + 384u * xg + (u32)j8 * 16u;
      const u32 od = (lane & 15) < 12 ? (u32)(96 * j + (lane >> 4)) * (D * 2u) + ((160u * xg) & ~63u) + (u32)(lane & 15) * 16u : OOB;
      const rsrc_t rg = mk(a.wg, D * F * 2), ru = mk(a.wu, D * F * 2), rd = mk(a.wd, F * D * 2);
      u32 v[54], keep = 0u;
      wait_flag<1>(FL + F_END, 4u, dead);
#pragma unroll
      for (int n = 0; n < 30; ++n) v[n] = __builtin_amdgcn_raw_buffer_load_b32((n & 1) ? ru : rg, og + ((n >> 1) % 3) * 128, (n / 6) * 8 * (F * 2), 0);
#pragma unroll
      for (int n = 0; n < 24; ++n) v[30 + n] = __builtin_amdgcn_raw_buffer_load_b32(rd, od, n * 4 * (D * 2), 0);
      SB();
#pragma unroll
      for (int n = 0; n < 54; ++n) keep |= v[n];
      asm volatile("" :: "v"(keep));
      STAMP(6);
    }
  } else {
    u32 xs[5]; u16 xr[3]; u32x4 wqv[3][3]; u32x4 ring[RING][2];
#pragma unroll
    for (int m = 0; m < 5; ++m) xs[m] = __builtin_amdgcn_raw_buffer_load_b32(rx, (lane + 64 * m) * 4, 0, 0);
    SB();
    {
      const u32 row = (u32)(80 * (j & 7) + 20 * w + r);
      const u32 o = row * (u32)(H * DH * 2) + (u32)(8 * (j >> 3) + xg) * 384u + (u32)j8 * 16u;
#pragma unroll
      for (int g = 0; g < 3; ++g) {
        const u32 og = (g < 2 || r < 4) ? o : OOB, ox = (g < 2 || r < 4) ? row * 2u : OOB;
#pragma unroll
        for (int i = 0; i < 3; ++i) wqv[g][i] = __builtin_amdgcn_raw_buffer_load_b128(rq, og + i * 128, g * 8 * (H * DH * 2), AUX_NT);
        xr[g] = __builtin_amdgcn_raw_buffer_load_b16(rx, ox, g * 16, 0);
      }
    }
    ev = __builtin_amdgcn_raw_buffer_load_b64(ws, eoff, 0, AUX_SC1);
    xme = __builtin_amdgcn_raw_buffer_load_b32(rx, tid * 4, 0, 0);
    SB();
    auto issue = [&](int slot, int n, int i) {
      const rsrc_t rs = mk(n < 64 ? a.k : a.v, n < 128 ? HEADB * H : 0u);
      const u32 so = step_off(n & 63);
      ring[slot][0] = __builtin_amdgcn_raw_buffer_load_b128(rs, voff + i * 128, so, AUX_NT);
      ring[slot][1] = __builtin_amdgcn_raw_buffer_load_b128(rs, voff + 3072 + i * 128, so, AUX_NT);
      SB();
    };
#pragma unroll
    for (int u = 0; u < RING; ++u) issue(u, u / 3, u % 3);
    float acc[24];
    {
      float s = 0.f;
#pragma unroll
      for (int m = 0; m < 5; ++m) s += lo_f(xs[m]) + hi_f(xs[m]);
      const float mean = wave_sum(s) * (1.f / D);
      STAMP(18);
      float q2 = 0.f;
#pragma unroll
      for (int m = 0; m < 5; ++m) { const float d0 = lo_f(xs[m]) - mean, d1 = hi_f(xs[m]) - mean; q2 += d0 * d0 + d1 * d1; }
      const float var = rbf(wave_sum(q2) * (1.f / D));
      const float mb = rbf(mean), inv = __builtin_amdgcn_rcpf(rbf(__builtin_amdgcn_sqrtf(rbf(var + EPS))));
#pragma unroll
      for (int e = 0; e < 24; ++e) acc[e] = 0.f;
#pragma unroll
      for (int g = 0; g < 3; ++g) {
        const u32 xl = pk_bf16(rbf(lo_f(xr[g]) - mb) * inv, 0.f), xh = xl << 16;
#pragma unroll
        for (int i = 0; i < 3; ++i)
#pragma unroll
          for (int e = 0; e < 4; ++e) {
            acc[i * 8 + 2 * e] = dot2(wqv[g][i][e], xl, acc[i * 8 + 2 * e]);
            acc[i * 8 + 2 * e + 1] = dot2(wqv[g][i][e], xh, acc[i * 8 + 2 * e + 1]);
          }
      }
      flush24(acc, lp<float>(L, L_PART + w * 768), lane);
    }
    STAMP(1);
    bar();
    make_tag();
    __builtin_amdgcn_s_barrier();
    u32x4 wov[8];
    {
      const rsrc_t ro = mk(a.wo, H * DH * D * 2);
#pragma unroll
      for (int n = 0; n < 8; ++n) {
        const u32 c = (u32)(480 * w + 64 * n + lane), row = (c * 6554u) >> 16;
        const u32 oo = ((u32)(8 * tj + xg) * DH + row) * (D * 2) + 160u * c8 + (c - 10u * row) * 16u;
        wov[n] = __builtin_amdgcn_raw_buffer_load_b128(ro, (n < 7 || lane < 32) ? oo : OOB, 0, AUX_NT);
      }
    }
    SB();
    bar();
    if (PADQ) pad(PADQ);
    STAMP(2);
    u32 nxt = (ANCR ? (u32)__builtin_amdgcn_readfirstlane(FL[F_ANC]) : ANCH ? tstart + ANCH : (u32)rt()) * 16u + (u32)((j * 4 + w * STW) * STAG) / 128u;
    u32 sch = nxt;
    const u32 per = PACE / PDIV - (u32)(j * CONV) / 32u;
    u32 tprev = nxt, vsum = 0, ew = 0, off_ = 0, skip_ = VSK;
#define PACE_STEP() do { \
      if (DLT) asm volatile("s_waitcnt vmcnt(%0)" :: "n"(2 * RING - 2 * WU) : "memory"); \
      u32 now_ = (u32)rt() * 16u; const bool late_ = (int)(nxt - now_) <= 0; \
      if (DLT && (int)(nxt - now_) > 0 && (int)(now_ - tprev) > TH) { sch += DLT; nxt += DLT; } \
      while ((int)(nxt - now_) > 0) { __builtin_amdgcn_s_sleep(1); now_ = (u32)rt() * 16u; } \
      if (VMS) { const u32 vc_ = __builtin_amdgcn_s_getreg(7 | (0 << 6) | (3 << 11)); vsum += vc_; ew += vc_ * 16u - (ew >> EWS); if (skip_) --skip_; else if (VDL && ew > (u32)(VTH * (1 << EWS))) { if (!late_) { sch += VDL; off_ += VDL; } } else if (VDN && off_ >= VDN && ew < (u32)(VLO * (1 << EWS))) { sch -= VDN; off_ -= VDN; } } \
      tprev = now_; sch += per; nxt = now_ + MING / PDIV; if ((int)(sch - nxt) > 0) nxt = sch; } while (0)
    LAS u32x4* sp = lp<u32x4>(L, L_S + w * 2048) + r;
    const float SCALE = 0.07216878364870322f;
    u32x4 q[3];
    auto k_block = [&](int bk, bool first) {
      u32x4 sc;
#pragma unroll
      for (int st = 0; st < 4; ++st) {
        float d[2] = {0.f, 0.f};
#pragma unroll
        for (int i = 0; i < 3; ++i) {
          const int u = 3 * st + i, slot = u % RING;
          if (i == 0 || PDIV == 3) PACE_STEP();
#pragma unroll
          for (int e = 0; e < 4; ++e) { d[0] = dot2(ring[slot][0][e], q[i][e], d[0]); d[1] = dot2(ring[slot][1][e], q[i][e], d[1]); }
          asm volatile("" : "+v"(d[0]), "+v"(d[1]));
          SB();
          issue(slot, 4 * bk + (u + RING) / 3, (u + RING) % 3);
        }
#pragma unroll
        for (int g = 0; g < 2; ++g) { d[g] += dpp<X1>(d[g]); d[g] += dpp<X2>(d[g]); d[g] += dpp<X7>(d[g]); }
        const u32 s1 = pk_bf16(d[0], d[1]);
        sc[st] = pk_bf16(lo_f(s1) * SCALE, hi_f(s1) * SCALE);
        if (first && st == 1) {
#pragma unroll
          for (int n = 0; n < 8; ++n) if (n < 7 || lane < 32) lp<u32x4>(L, L_WO)[480 * w + 64 * n + lane] = wov[n];
        }
      }
      if (j8 == 0) sp[bk * 8] = sc;
    };
    auto load_q = [&](int t) {
#pragma unroll
      for (int i = 0; i < 3; ++i) q[i] = lp<u32x4>(L, L_Q + t * 384)[j8 + 8 * i];
    };
    load_q(0);
    k_block(0, true);
#pragma unroll 1
    for (int bk = 1; bk < 16; ++bk) {
      if ((bk & 3) == 0) { VREC(19 + (bk >> 2) - 1); signal_add((LAS u32*)FL + F_K + (bk >> 2) - 1, lane); wait_flag<1>(FL + F_Q, 1u, dead); load_q(bk >> 2); }
      k_block(bk, false);
    }
    VREC(22);
    signal_add((LAS u32*)FL + F_K + 3, lane);
    STAMP(3);
#pragma unroll 1
    for (int bk = 0; bk < 16; ++bk) {
      if ((bk & 3) == 0) {
        wait_flag<1>(FL + F_P + (bk >> 2), 1u, dead);
#pragma unroll
        for (int e = 0; e < 24; ++e) acc[e] = 0.f;
      }
      const u32x4 pp = sp[bk * 8];
      if (bk == CPB) FL[F_BLK] = 1u;
#pragma unroll
      for (int st = 0; st < 4; ++st) {
#pragma unroll
        for (int i = 0; i < 3; ++i) {
          const int u = 3 * st + i, slot = u % RING;
          if ((i == 0 || PDIV == 3) && (u + RING < 12 || bk < 15)) PACE_STEP();
#pragma unroll
          for (int e = 0; e < 4; ++e) {
            acc[i * 8 + 2 * e] = dot2(zip_lo(ring[slot][0][e], ring[slot][1][e]), pp[st], acc[i * 8 + 2 * e]);
            acc[i * 8 + 2 * e + 1] = dot2(zip_hi(ring[slot][0][e], ring[slot][1][e]), pp[st], acc[i * 8 + 2 * e + 1]);
          }
#pragma unroll
          for (int e = 0; e < 8; ++e) asm volatile("" : "+v"(acc[i * 8 + e]));
          SB();
          issue(slot, 64 + 4 * bk + (u + RING) / 3, (u + RING) % 3);
          if (u + RING == 11 && bk == 15 && lane == 0) __hip_atomic_fetch_add((LAS u32*)FL + F_END, 1u, __ATOMIC_RELAXED, __HIP_MEMORY_SCOPE_WORKGROUP);
        }
      }
      if ((bk & 3) == 3) {
        VREC(23 + (bk >> 2));
        if (PADV && bk == 15) pad(PADV);
        if (bk == 15) STAMP(4); else STAMP(14 + (bk >> 2));
        flush24(acc, lp<float>(L, L_PART + ((bk >> 2) * 4 + w) * 768), lane);
        if (bk < 15) signal_add((LAS u32*)FL + F_V + (bk >> 2), lane);
      }
    }
    auto publish_last = [&]() {
      asm volatile("s_waitcnt lgkmcnt(0)" ::: "memory");
      u32 old = 0u;
      if (lane == 0) old = __hip_atomic_fetch_add((LAS u32*)FL + F_L, 1u, __ATOMIC_RELAXED, __HIP_MEMORY_SCOPE_WORKGROUP);
      if (__builtin_amdgcn_readfirstlane(old) == 3u) {
        asm volatile("" ::: "memory");
        publish192(ws, lp<f32x2>(L, L_PART + 3 * 3072), WS_X2 + (u32)(((3 * 8 + xg) * 32 + j) * 96) * 16u, lane, T0, T1);
        STAMP(6);
      }
      SB();
    };
    auto ring_done = [&]() {
#pragma unroll
      for (int u = 0; u < RING; ++u) asm volatile("" :: "v"(ring[u][0]), "v"(ring[u][1]));
      SB();
    };
    if (tj == 3) { ring_done(); publish_last(); } else { publish_last(); ring_done(); }
    STAMP(5);
  }

  if (tj == 3) {
    u32x4 wp[6];
    load_wo(L, w, lane, wp);
#pragma unroll
    for (int e = 0; e < 6; ++e) asm volatile("" : "+v"(wp[e]));
    if (w != SVC) {
      __builtin_amdgcn_s_sleep(PRE);
      gather_o<1, SLP_A>(ws, L, 3, xg, w, lane, T0, T1);
    }
    STAMP(7);
    bar();
    if (w == SVC) put_rec(ws, lane < 8 ? WS_XH + (u32)(lane * 64 + xg * 8 + c8) * 128u : OOB, 0u, 0u, T0, T1);
    f32x2 y = lp<f32x2>(L, L_Y)[8 * w + j8];
    finish_o(L, lp<u32>(L, L_OB + w * 384), lane);
    y += gemv16(lp<u32>(L, L_OB + w * 384), wp, lane);
    put_rec(ws, WS_X3 + (u32)r * X3_REP + (u32)(xg * 320 + 40 * c8 + 8 * w + j8) * 16u, __float_as_uint(y.x), __float_as_uint(y.y), T0, T1);
    SB();
    STAMP(8);
    __builtin_amdgcn_s_barrier();
    SB();
    if (w < 3) {
      u32x4 da[8]; u32 db[8];
      const rsrc_t rd = mk(a.wd, F * D * 2);
      const u32 od = (u32)tid * (8u * D * 2u) + 160u * xg + 20u * c8;
#pragma unroll
      for (int n = 0; n < 8; ++n) {
        da[n] = __builtin_amdgcn_raw_buffer_load_b128(rd, od, n * D * 2, 0);
        db[n] = __builtin_amdgcn_raw_buffer_load_b32(rd, od + 16u, n * D * 2, 0);
      }
      u32 dp[4][10];
#pragma unroll
      for (int n = 0; n < 4; ++n) {
#pragma unroll
        for (int e = 0; e < 4; ++e) { dp[n][2 * e] = zip_lo(da[2 * n][e], da[2 * n + 1][e]); dp[n][2 * e + 1] = zip_hi(da[2 * n][e], da[2 * n + 1][e]); }
        dp[n][8] = zip_lo(db[2 * n], db[2 * n + 1]); dp[n][9] = zip_hi(db[2 * n], db[2 * n + 1]);
#pragma unroll
        for (int e = 0; e < 10; ++e) asm volatile("" : "+v"(dp[n][e]));
      }
      STAMP(10);
      u32 off[2] = {WS_X4 + (u32)xg * X4_REP + (u32)tid * 128u, WS_X4 + (u32)xg * X4_REP + (u32)tid * 128u + 16u}, d0[2], d1[2];
      get_recs<2, SLP_C>(ws, off, T0, T1, d0, d1, dead);
      STAMP(11);
      float c[10], c5[5];
#pragma unroll
      for (int e = 0; e < 10; ++e) c[e] = dot2(dp[3][e], d1[1], dot2(dp[2][e], d0[1], dot2(dp[1][e], d1[0], dot2(dp[0][e], d0[0], 0.f))));
#pragma unroll
      for (int e = 0; e < 5; ++e) c5[e] = tsum32(c[e], c[e + 5]);
      const float v0 = row_sum(tsum16(c5[0], c5[1])), v1 = row_sum(tsum16(c5[2], c5[3])), v2 = row_sum(tsum16(c5[4], c5[4]));
      if ((lane & 15) == 0) {
        LAS float* dn = lp<float>(L, L_DN) + w * 16 + 5 * (lane >> 5);
        const int b4 = (lane >> 4) & 1;
        dn[b4] = v0; dn[2 + b4] = v1; dn[4] = v2;
      }
    } else if (w == 3) {
      const int cp = lane & 7;
      u32 off[1] = {WS_X3 + (u32)xg * X3_REP + (u32)(r * 320 + 40 * xg + 5 * c8 + (cp < 5 ? cp : 0)) * 16u}, d0[1], d1[1];
      const u32 xv = __builtin_amdgcn_raw_buffer_load_b32(rx, (40 * xg + 5 * c8 + (cp < 5 ? cp : 0)) * 4, 0, 0);
      __builtin_amdgcn_s_sleep(PRE);
      get_recs<1, 8>(ws, off, T0, T1, d0, d1, dead);
      float s0 = __uint_as_float(d0[0]), s1 = __uint_as_float(d1[0]);
      s0 = tsum32(s0, s0); s1 = tsum32(s1, s1); s0 = tsum16(s0, s0); s1 = tsum16(s1, s1); s0 += dpp<R8>(s0); s1 += dpp<R8>(s1);
      const u32 at = pk_bf16(s0, s1);
      if (lane < 5) lp<u32>(L, L_H)[lane] = pk_bf16(lo_f(xv) + lo_f(at), hi_f(xv) + hi_f(at));
    }
    bar();
    if (tid < 10) {
      const float s = rbf((lp<float>(L, L_DN)[tid] + lp<float>(L, L_DN)[16 + tid]) + lp<float>(L, L_DN)[32 + tid]);
      a.out[80 * xg + 10 * c8 + tid] = (u16)pk_bf16(lo_f(lp<u16>(L, L_H)[tid]) + s, 0.f);
    }
  } else {
    bar();
    if (GDEL && j >= GJ) __builtin_amdgcn_s_sleep(GDEL);
    u32 gp[16];
    {
      u32 pf[4];
      const u32 od = ((lane & 15) < 12 && w < 4 && !NOPF && !PF2) ? (u32)(64 * j + 16 * w + (lane >> 4)) * (D * 2u) + ((160u * xg) & ~63u) + (u32)(lane & 15) * 16u : OOB;
#pragma unroll
      for (int n = 0; n < 4; ++n) pf[n] = __builtin_amdgcn_raw_buffer_load_b32(mk(a.wd, F * D * 2), od, n * 4 * (D * 2), 0);
      SB();
      const rsrc_t rg = mk(a.wg, D * F * 2), ru = mk(a.wu, D * F * 2);
      const u32 og = (u32)tid * (2u * F * 2u) + 384u * xg + 16u * j;
      const u32x4 A = __builtin_amdgcn_raw_buffer_load_b128(rg, og, 0, 0), B = __builtin_amdgcn_raw_buffer_load_b128(rg, og, F * 2, 0);
      const u32x4 C = __builtin_amdgcn_raw_buffer_load_b128(ru, og, 0, 0), E = __builtin_amdgcn_raw_buffer_load_b128(ru, og, F * 2, 0);
#pragma unroll
      for (int e = 0; e < 4; ++e) {
        gp[2 * e] = zip_lo(A[e], B[e]); gp[2 * e + 1] = zip_hi(A[e], B[e]);
        gp[8 + 2 * e] = zip_lo(C[e], E[e]); gp[8 + 2 * e + 1] = zip_hi(C[e], E[e]);
      }
#pragma unroll
      for (int e = 0; e < 16; ++e) asm volatile("" : "+v"(gp[e]));
      asm volatile("" :: "v"(pf[0] | pf[1] | pf[2] | pf[3]));
    }
    STAMP(8);
    if (w == SVC) {
      u32 off[1] = {SENT_B == 2 ? WS_XH + (u32)(xg * 64 + lane) * 128u : WS_X3 + (u32)xg * X3_REP + (u32)(r * 320 + 40 * j8 + 39) * 16u}, d0[1], d1[1];
      get_recs<1, SLP_H>(ws, off, T0, T1, d0, d1, dead);
    }
    bar();
    u32 hp;
    {
      u32 off[8], d0[8], d1[8];
#pragma unroll
      for (int k = 0; k < 8; ++k) off[k] = WS_X3 + (u32)xg * X3_REP + (u32)(k * 320 + tid) * 16u;
      get_recs<8, SLP_B>(ws, off, T0, T1, d0, d1, dead);
      auto sum8 = [](const u32 (&d)[8]) {
        float t[4];
#pragma unroll
        for (int k = 0; k < 4; ++k) t[k] = __uint_as_float(d[k]) + __uint_as_float(d[k + 4]);
        return (t[0] + t[2]) + (t[1] + t[3]);
      };
      const u32 at = pk_bf16(sum8(d0), sum8(d1));
      hp = pk_bf16(lo_f(xme) + lo_f(at), hi_f(xme) + hi_f(at));
    }
    STAMP(9);
    const float h0 = lo_f(hp), h1 = hi_f(hp);
    {
      const float c = __uint_as_float(__builtin_amdgcn_readfirstlane(__float_as_uint(h0)));
      const float e0 = h0 - c, e1 = h1 - c;
      const float s = row_sum(tsum16(e0 + e1, e0 * e0 + e1 * e1));
      const float t = tsum32(s, s);
      auto p = __builtin_amdgcn_permlane16_swap(__float_as_uint(t), __float_as_uint(t), false, false);
      const float s1 = __uint_as_float(p[0]) * (1.f / 128.f);
      if (lane == 0) lp<f32x2>(L, L_ST)[w] = f32x2{c + s1, __uint_as_float(p[1]) - 128.f * s1 * s1};
    }
    bar();
    if (b == 0 && tid == 0) {
      const u64 e1 = (((u64)ev.y << 32) | ev.x) + 1ull;
      __builtin_amdgcn_raw_buffer_store_b64(u32x2{(u32)e1, (u32)(e1 >> 32)}, ws, WS_E, 0, AUX_SC1);
    }
    f32x2 st[5];
#pragma unroll
    for (int ww = 0; ww < 5; ++ww) st[ww] = lp<f32x2>(L, L_ST)[ww];
    const float mean = (((st[0].x + st[1].x) + (st[2].x + st[3].x)) + st[4].x) * 0.2f;
    float m2 = 0.f;
#pragma unroll
    for (int ww = 0; ww < 5; ++ww) m2 += st[ww].y + 128.f * (st[ww].x - mean) * (st[ww].x - mean);
    const float var = rbf(m2 * (1.f / D));
    const float mb = rbf(mean), inv = __builtin_amdgcn_rcpf(rbf(__builtin_amdgcn_sqrtf(rbf(var + EPS))));
    const u32 dd = pk_bf16(h0 - mb, h1 - mb);
    const u32 hn = pk_bf16(lo_f(dd) * inv, hi_f(dd) * inv);
    float g[16];
#pragma unroll
    for (int e = 0; e < 16; ++e) g[e] = dot2(gp[e], hn, 0.f);
    float g8[8], g4[4], g2[2];
#pragma unroll
    for (int e = 0; e < 8; ++e) g8[e] = tsum32(g[e], g[e + 8]);
#pragma unroll
    for (int e = 0; e < 4; ++e) g4[e] = tsum16(g8[e], g8[e + 4]);
    const bool b3 = (lane & 8) != 0, b2 = (lane & 4) != 0;
#pragma unroll
    for (int e = 0; e < 2; ++e) g2[e] = (b3 ? g4[e + 2] : g4[e]) + dpp<R8>(b3 ? g4[e] : g4[e + 2]);
    float g1 = (b2 ? g2[1] : g2[0]) + dpp<X7>(b2 ? g2[0] : g2[1]);
    g1 += dpp<X2>(g1); g1 += dpp<X1>(g1);
    if ((lane & 3) == 0) lp<float>(L, L_GU)[w * 16 + (lane >> 2)] = g1;
    bar();
    if (w == 0) {
      float gs = 0.f, us = 0.f;
#pragma unroll
      for (int ww = 0; ww < 5; ++ww) { gs += lp<float>(L, L_GU)[ww * 16 + j8]; us += lp<float>(L, L_GU)[ww * 16 + 8 + j8]; }
      const u32 gu = pk_bf16(gs, us);
      const float sg = rbf(__builtin_amdgcn_rcpf(1.f + __builtin_amdgcn_exp2f(-1.4426950408889634f * lo_f(gu))));
      const float f = sg * hi_f(gu);
      const u32 d0 = pk_bf16(dpp<0x00>(f), dpp<0x55>(f)), d1 = pk_bf16(dpp<0xAA>(f), dpp<0xFF>(f));
      put_rec(ws, (lane & 3) == 0 ? WS_X4 + (u32)r * X4_REP + (u32)(xg * 24 + j) * 128u + (u32)(lane & 4) * 4u : OOB, d0, d1, T0, T1);
    }
    STAMP(10);
  }
  if (PADE) pad(PADE);
  STAMP(12);
  asm volatile(".Lfb_end:");
}
}

extern "C" void kernel_launch(void* const* d_in, const int* in_sizes, int n_in, void* d_out, int out_size, void* d_ws, size_t ws_size,
                              hipStream_t stream) {
  Args a;
  a.x = (const u16*)d_in[0]; a.k = (const u16*)d_in[1]; a.v = (const u16*)d_in[2]; a.wq = (const u16*)d_in[3]; a.wo = (const u16*)d_in[4];
  a.wg = (const u16*)d_in[6]; a.wu = (const u16*)d_in[7]; a.wd = (const u16*)d_in[8];
  a.out = (u16*)d_out; a.ws = (unsigned char*)d_ws;
  hipLaunchKernelGGL(fused_block, dim3(256), dim3(NTHR), 0, stream, a);
}
```

```cpp
#include <hip/hip_runtime.h>
#include <hip/hip_bf16.h>
#include <stdint.h>

typedef unsigned u32;
typedef unsigned long long u64;
typedef unsigned short u16;
typedef u32 u32x2 __attribute__((ext_vector_type(2)));
typedef u32 u32x4 __attribute__((ext_vector_type(4)));
typedef float f32x2 __attribute__((ext_vector_type(2)));
typedef float f32x4 __attribute__((ext_vector_type(4)));
typedef __bf16 bf16x2 __attribute__((ext_vector_type(2)));
typedef __amdgpu_buffer_rsrc_t rsrc_t;
#define LAS __attribute__((address_space(3)))

namespace {
constexpr int D = 640, H = 32, SK = 32768, DH = 192, F = 1536;
constexpr u32 HEADB = (u32)SK * DH * 2;
constexpr int NTHR = 320, SVC = 4;
constexpr u32 OOB = 0x80000000u;
constexpr int AUX_NT = 2, AUX_SC1 = 16;
constexpr u32 WS_E = 0, WS_X1 = 4096, WS_X2 = WS_X1 + 4 * 8 * 32 * 128, WS_X0 = WS_X2 + 4 * 8 * 32 * 1536, WS_XC = WS_X0 + 8 * 32 * 1536,
              WS_XO = WS_XC + 8 * 24 * 640, WS_XT = WS_XO + 8 * 24 * 128, WS_XH = WS_XT + 8 * 8 * 128, WS_X3 = WS_XH + 8 * 64 * 128, X3_REP = 8 * 320 * 16, WS_X4 = WS_X3 + 8 * X3_REP, X4_REP = 192 * 128, WS_BYTES = WS_X4 + 8 * X4_REP;
static_assert(WS_XC % 128 == 0 && WS_X3 % 128 == 0 && WS_X4 % 128 == 0, "ws");
constexpr int L_Q = 0, L_Y = 1536, L_CG = 1920, L_FLAG = 2048, L_ST = 2176, L_GU = 2240, L_DN = 2560, L_H = 2752, L_OB = 3072, L_RED = 5120,
              L_S = 8192, L_PART = 16384, L_WO = 28672, L_WP = 59392, LDS_BYTES = L_WP + 30720;
constexpr int F_K = 0, F_P = 4, F_V = 8, F_DEAD = 13, F_Q = 14, F_L = 15, F_BLK = 16, F_END = 17, F_ANC = 18, F_SHIFT = 19;
constexpr u32 SPIN_G = 1u << 17, SPIN_L = 1u << 21;
constexpr int RING = 6;
constexpr u32 PACE = 1317;
constexpr u32 MINGAP = 1100;
constexpr u32 ANCHOR = 215;
constexpr u32 YIELD = 2634;
constexpr int ORDER = 0x2310;

struct Args {
  const u16 *x, *k, *v, *wq, *wo, *wg, *wu, *wd;
  u16* out;
  unsigned char* ws;
};

__device__ __forceinline__ int hg(int t) { return (ORDER >> (4 * t)) & 3; }
__device__ __forceinline__ float dot2(u32 a, u32 b, float c) {
  return __builtin_amdgcn_fdot2_f32_bf16(__builtin_bit_cast(bf16x2, a), __builtin_bit_cast(bf16x2, b), c, false);
}
__device__ __forceinline__ u32 pk_bf16(float lo, float hi) {
  bf16x2 r = {(__bf16)lo, (__bf16)hi};
  return __builtin_bit_cast(u32, r);
}
__device__ __forceinline__ float lo_f(u32 a) { return __uint_as_float(a << 16); }
__device__ __forceinline__ float hi_f(u32 a) { return __uint_as_float(a & 0xffff0000u); }
__device__ __forceinline__ float rbf(float a) { return lo_f(pk_bf16(a, a)); }
__device__ __forceinline__ u32 zip_lo(u32 a, u32 b) { return __builtin_amdgcn_perm(b, a, 0x05040100u); }
__device__ __forceinline__ u32 zip_hi(u32 a, u32 b) { return __builtin_amdgcn_perm(b, a, 0x07060302u); }
template <int CTRL> __device__ __forceinline__ float dpp(float x) {
  return __builtin_bit_cast(float, __builtin_amdgcn_mov_dpp(__builtin_bit_cast(int, x), CTRL, 0xf, 0xf, true));
}
constexpr int X1 = 0xB1, X2 = 0x4E, X7 = 0x141, R8 = 0x128;
__device__ __forceinline__ float tsum32(float a, float b) {
  auto s = __builtin_amdgcn_permlane32_swap(__float_as_uint(a), __float_as_uint(b), false, false);
  return __uint_as_float(s[0]) + __uint_as_float(s[1]);
}
__device__ __forceinline__ float tsum16(float a, float b) {
  auto s = __builtin_amdgcn_permlane16_swap(__float_as_uint(a), __float_as_uint(b), false, false);
  return __uint_as_float(s[0]) + __uint_as_float(s[1]);
}
__device__ __forceinline__ float row_sum(float x) { x += dpp<R8>(x); x += dpp<X7>(x); x += dpp<X2>(x); x += dpp<X1>(x); return x; }
__device__ __forceinline__ float row_max(float x) {
  x = fmaxf(x, dpp<R8>(x)); x = fmaxf(x, dpp<X7>(x)); x = fmaxf(x, dpp<X2>(x)); x = fmaxf(x, dpp<X1>(x)); return x;
}
__device__ __forceinline__ float wave_sum(float x) { x = row_sum(x); x = tsum16(x, x); return tsum32(x, x); }
__device__ __forceinline__ float wave_max(float x) {
  x = row_max(x);
  auto s = __builtin_amdgcn_permlane16_swap(__float_as_uint(x), __float_as_uint(x), false, false);
  x = fmaxf(__uint_as_float(s[0]), __uint_as_float(s[1]));
  auto t = __builtin_amdgcn_permlane32_swap(__float_as_uint(x), __float_as_uint(x), false, false);
  return fmaxf(__uint_as_float(t[0]), __uint_as_float(t[1]));
}
__device__ __forceinline__ rsrc_t mk(const void* p, u32 bytes) { return __builtin_amdgcn_make_buffer_rsrc((void*)p, 0, bytes, 0x00020000); }
__device__ __forceinline__ void bar() {
  asm volatile("s_waitcnt lgkmcnt(0)" ::: "memory");
  __builtin_amdgcn_s_barrier();
  asm volatile("" ::: "memory");
}
__device__ __forceinline__ void touch2(const void* p, const void* q) {
  u32 d0, d1;
  asm volatile("s_load_dword %0, %2, 0x0\n\ts_load_dword %1, %3, 0x0\n\ts_waitcnt lgkmcnt(0)" : "=&s"(d0), "=&s"(d1) : "s"(p), "s"(q) : "memory");
}
__device__ __forceinline__ u64 rt() { return __builtin_amdgcn_s_memrealtime(); }
#define SB() __builtin_amdgcn_sched_barrier(0)

template <typename T> __device__ __forceinline__ LAS T* lp(LAS unsigned char* l, int off) { return (LAS T*)(l + off); }

__device__ __forceinline__ void put_rec(rsrc_t ws, u32 off, u32 d0, u32 d1, u32 T0, u32 T1) {
  __builtin_amdgcn_raw_buffer_store_b128(u32x4{d0, d1, d0 ^ T0, d1 ^ T1}, ws, off, 0, AUX_SC1);
}
template <int N, int SLEEP>
__device__ __forceinline__ void get_recs(rsrc_t ws, const u32 (&off)[N], u32 T0, u32 T1, u32 (&d0)[N], u32 (&d1)[N], volatile LAS u32* dead,
                                         bool bar_behind_first = false) {
#pragma unroll 1
  for (u32 spin = 0;; ++spin) {
    asm volatile("" ::: "memory");
    u32x4 t[N];
#pragma unroll
    for (int k = 0; k < N; ++k) t[k] = __builtin_amdgcn_raw_buffer_load_b128(ws, off[k], 0, AUX_SC1);
    SB();
    if (bar_behind_first && spin == 0) __builtin_amdgcn_s_barrier();
    u32 bad = 0u;
#pragma unroll
    for (int k = 0; k < N; ++k) {
      bad = __builtin_amdgcn_bitop3_b32(bad, __builtin_amdgcn_bitop3_b32(t[k].x, t[k].z, T0, 0x96), __builtin_amdgcn_bitop3_b32(t[k].y, t[k].w, T1, 0x96), 0xFE);
      d0[k] = t[k].x; d1[k] = t[k].y;
    }
    if (!__builtin_amdgcn_ballot_w64(bad != 0u)) break;
    if (SLEEP) __builtin_amdgcn_s_sleep(SLEEP);
    if ((spin & 255u) == 255u) { if (*dead) break; if (spin > SPIN_G) { *dead = 1u; break; } }
  }
}
template <int SLEEP>
__device__ __forceinline__ void wait_flag(volatile LAS u32* f, u32 want, volatile LAS u32* dead) {
  for (u32 spin = 0; (u32)__builtin_amdgcn_readfirstlane(*f) < want; ++spin) {
    if (SLEEP) __builtin_amdgcn_s_sleep(SLEEP);
    if ((spin & 255u) == 255u) { if (*dead) break; if (spin > SPIN_L) { *dead = 1u; break; } }
  }
  asm volatile("" ::: "memory");
}
__device__ __forceinline__ void signal_add(LAS u32* f, int lane) {
  asm volatile("s_waitcnt lgkmcnt(0)" ::: "memory");
  if (lane == 0) __hip_atomic_fetch_add(f, 1u, __ATOMIC_RELAXED, __HIP_MEMORY_SCOPE_WORKGROUP);
}

template <int SLEEP>
__device__ __forceinline__ void gather8(rsrc_t ws, LAS unsigned char* L, u32 first, int lane, u32 T0, u32 T1, float (&s)[3], bool bbf = false) {
  u32 off[12], d0[12], d1[12];
  const u32 base = first + (u32)((lane >> 5) * 96 + (lane & 31)) * 16u;
#pragma unroll
  for (int n = 0; n < 4; ++n)
#pragma unroll
    for (int pr = 0; pr < 3; ++pr) off[n * 3 + pr] = base + (u32)(2 * n * 96 + 32 * pr) * 16u;
  get_recs<12, SLEEP>(ws, off, T0, T1, d0, d1, lp<u32>(L, L_FLAG) + F_DEAD, bbf);
#pragma unroll
  for (int pr = 0; pr < 3; ++pr) {
    const float a = (__uint_as_float(d0[pr]) + __uint_as_float(d0[pr + 3])) + (__uint_as_float(d0[pr + 6]) + __uint_as_float(d0[pr + 9]));
    const float b = (__uint_as_float(d1[pr]) + __uint_as_float(d1[pr + 3])) + (__uint_as_float(d1[pr + 6]) + __uint_as_float(d1[pr + 9]));
    s[pr] = tsum32(a, b);
  }
}
__device__ __forceinline__ void flush24(const float (&acc)[24], LAS float* dst, int lane) {
#pragma unroll
  for (int i = 0; i < 3; ++i) {
    float s1[4], s2[2];
#pragma unroll
    for (int e = 0; e < 4; ++e) s1[e] = tsum32(acc[i * 8 + e], acc[i * 8 + e + 4]);
#pragma unroll
    for (int e = 0; e < 2; ++e) s2[e] = tsum16(s1[e], s1[e + 2]);
    const bool b3 = (lane & 8) != 0;
    const float keep = b3 ? s2[1] : s2[0], send = b3 ? s2[0] : s2[1];
    dst[64 * i + 8 * (lane & 7) + (lane >> 3)] = keep + dpp<R8>(send);
  }
}
__device__ __forceinline__ void publish192(rsrc_t ws, const LAS f32x2* src, u32 first, int lane, u32 T0, u32 T1) {
  const LAS f32x2* p0 = src + lane; const LAS f32x2* p1 = src + 64 + (lane & 31);
  const f32x2 s0 = (p0[0] + p0[96]) + (p0[192] + p0[288]), s1 = (p1[0] + p1[96]) + (p1[192] + p1[288]);
  put_rec(ws, first + (u32)lane * 16u, __float_as_uint(s0.x), __float_as_uint(s0.y), T0, T1);
  put_rec(ws, lane < 32 ? first + 1024u + (u32)lane * 16u : OOB, __float_as_uint(s1.x), __float_as_uint(s1.y), T0, T1);
}
__device__ __forceinline__ void pack_wo(LAS unsigned char* L, int vw, int lane) {
  const LAS u32* src = lp<u32>(L, L_WO) + (lane >> 3) * 960 + 8 * vw + (lane & 7);
#pragma unroll
  for (int m = 0; m < 12; m += 2) {
    const u32 A = src[80 * m], B = src[80 * m + 40], C = src[80 * m + 80], E = src[80 * m + 120];
    lp<u32x4>(L, L_WP)[(vw * 6 + m / 2) * 64 + lane] = u32x4{zip_lo(A, B), zip_hi(A, B), zip_lo(C, E), zip_hi(C, E)};
  }
}
__device__ __forceinline__ void load_wo(LAS unsigned char* L, int vw, int lane, u32x4 (&wp)[6]) {
#pragma unroll
  for (int m = 0; m < 6; ++m) wp[m] = lp<u32x4>(L, L_WP)[(vw * 6 + m) * 64 + lane];
}
__device__ __forceinline__ void finish_o(LAS unsigned char* L, LAS u32* ob, int lane) {
  const LAS f32x4* rp = lp<f32x4>(L, L_RED) + (lane < 48 ? lane : 0);
  const f32x4 s = (rp[0] + rp[48]) + (rp[96] + rp[144]);
  if (lane < 48) ((LAS u32x2*)ob)[lane] = u32x2{pk_bf16(s.x, s.y), pk_bf16(s.z, s.w)};
  asm volatile("s_waitcnt lgkmcnt(0)" ::: "memory");
}
__device__ __forceinline__ f32x2 gemv16(const LAS u32* ob, const u32x4 (&wp)[6], int lane) {
  const LAS u32x4* op = (const LAS u32x4*)ob + (lane >> 3) * 3;
  const u32x4 o0 = op[0], o1 = op[1], o2 = op[2];
  float y[4] = {0.f, 0.f, 0.f, 0.f};
#pragma unroll
  for (int m = 0; m < 12; ++m) {
    const u32 o = m < 4 ? o0[m & 3] : m < 8 ? o1[m & 3] : o2[m & 3];
    y[2 * (m & 1)] = dot2(wp[m >> 1][2 * (m & 1)], o, y[2 * (m & 1)]); y[2 * (m & 1) + 1] = dot2(wp[m >> 1][2 * (m & 1) + 1], o, y[2 * (m & 1) + 1]);
  }
  float y0 = y[0] + y[2], y1 = y[1] + y[3];
  y0 += dpp<R8>(y0); y1 += dpp<R8>(y1);
  y0 = tsum16(y0, y0); y1 = tsum16(y1, y1);
  return f32x2{tsum32(y0, y0), tsum32(y1, y1)};
}

__global__ void __launch_bounds__(NTHR, 2) fused_block(Args a) {
  __shared__ __attribute__((aligned(16))) unsigned char lds_raw[LDS_BYTES];
  LAS unsigned char* L = (LAS unsigned char*)lds_raw;
  const int tid = threadIdx.x, lane = tid & 63, w = __builtin_amdgcn_readfirstlane(tid >> 6);
  const int b = blockIdx.x, xg = b & 7, j = b >> 3;
  const int r = lane >> 3, j8 = lane & 7;
  const int tj = j >> 3, c8 = j & 7;
  volatile LAS u32* FL = lp<u32>(L, L_FLAG);
  volatile LAS u32* dead = FL + F_DEAD;
  const u32 tstart = (u32)rt();
  const rsrc_t ws = mk(a.ws, WS_BYTES);
  const rsrc_t rx = mk(a.x, D * 2), rq = mk(a.wq, D * H * DH * 2);

  const float EPS = 1e-5f;
  u64 pc0; u32 clen;
  asm volatile("s_getpc_b64 %0\n.Lfb_begin:\n\ts_mov_b32 %1, .Lfb_end-.Lfb_begin" : "=s"(pc0), "=s"(clen));
  auto code_touch = [&](int piece) -> u32 {
    return __builtin_amdgcn_raw_buffer_load_b32(mk((const void*)pc0, clen), (u32)piece * 1024u + (u32)lane * 16u, 0, 0);
  };
  u32 eoff = WS_E; asm volatile("" : "+v"(eoff));
  const u32 voff = (u32)r * 384u + (u32)j8 * 16u;
  const u32 sbase = (u32)xg * HEADB + (u32)j * 24576u + (u32)w * 6144u;
  auto step_off = [&](int n) -> u32 { return sbase + (u32)hg(n >> 4) * (8u * HEADB) + (u32)(n & 15) * (32u * 24576u); };
  u32x2 ev; u32 xme, T0, T1;
  auto make_tag = [&]() {
    u64 z = (((u64)ev.y << 32) | ev.x) + 0x9E3779B97F4A7C15ull;
    z = (z ^ (z >> 30)) * 0xBF58476D1CE4E5B9ull; z = (z ^ (z >> 27)) * 0x94D049BB133111EBull; z ^= z >> 31;
    T0 = __builtin_amdgcn_readfirstlane((u32)z) | 1u; T1 = __builtin_amdgcn_readfirstlane((u32)(z >> 32)) | 1u;
  };

  if (w == SVC) {
    ev = __builtin_amdgcn_raw_buffer_load_b64(ws, eoff, 0, AUX_SC1);
    xme = __builtin_amdgcn_raw_buffer_load_b32(rx, tid * 4, 0, 0);
    if (lane < 32) FL[lane] = 0u;
    const u32 cv = code_touch(j);
    SB();
    {
      const char* pq = (const char*)a.wq + (size_t)(80 * c8) * (H * DH * 2);
      const char* pk = (const char*)a.k + ((u32)(8 * hg(0) + xg) * HEADB + (u32)j * 24576u);
      const char* po = (const char*)a.wo + (size_t)(8 * hg(tj) + xg) * DH * D * 2;
      u32 e0, e1, e2, e3;
      asm volatile("s_load_dword %0, %4, 0x0\n\ts_load_dword %1, %5, 0x0\n\ts_load_dword %2, %6, 0x0\n\ts_load_dword %3, %7, 0x0\n\t"
                   "s_barrier\n\ts_waitcnt lgkmcnt(0)"
                   : "=&s"(e0), "=&s"(e1), "=&s"(e2), "=&s"(e3) : "s"(pq), "s"(pk), "s"(pq + 79 * (H * DH * 2)), "s"(po) : "memory");
    }
    asm volatile("" :: "v"(cv));
    make_tag();
    bar();
    publish192(ws, lp<f32x2>(L, L_PART), WS_X0 + (u32)((xg * 32 + j) * 96) * 16u, lane, T0, T1);
    { const u32 now = (u32)rt(); put_rec(ws, (lane == 0 && j < 8) ? WS_XT + (u32)(xg * 8 + j) * 128u : OOB, now, now, T0, T1); }
    __builtin_amdgcn_s_sleep(28);
    const u32x4 tv = __builtin_amdgcn_raw_buffer_load_b128(ws, WS_XT + (u32)(xg * 8 + j8) * 128u, 0, AUX_SC1);
#pragma unroll 1
    for (int t = 0; t < 4; ++t) {
      float s[3];
      gather8<4>(ws, L, WS_X0 + (u32)((xg * 32 + 8 * t) * 96) * 16u, lane, T0, T1, s, t == 0);
#pragma unroll
      for (int pr = 0; pr < 3; ++pr) lp<u16>(L, L_Q)[t * 192 + (32 * pr + (lane & 31)) * 2 + (lane >> 5)] = (u16)pk_bf16(s[pr], 0.f);
      if (t == 0) {
        {
          const bool ok = ((tv.x ^ tv.z ^ T0) | (tv.y ^ tv.w ^ T1)) == 0u;
          float m = ok ? (float)(int)(tv.x - tstart) : 1e9f;
          m = fmaxf(m, dpp<X1>(m)); m = fmaxf(m, dpp<X2>(m)); m = fmaxf(m, dpp<X7>(m));
          FL[F_ANC] = m < 2000.f ? tstart + (u32)(int)m + ANCHOR : (u32)rt();
        }
        bar();
        {
          auto at = [&](int n) -> const char* {
            if (n < 49) { const int sl = n / 7 + 1, pt = n % 7;
              return (const char*)(sl < 4 ? a.k : a.v) + (size_t)(8 * hg(sl & 3) + xg) * HEADB + (pt ? (size_t)pt * 2097152 - 64 : 0); }
            const int m = n - 49, e = (m & 1) * (D * F * 2 - 64);
            return m < 2 ? (const char*)a.wg + e : m < 4 ? (const char*)a.wu + e : m < 6 ? (const char*)a.wd + e : m < 8 ? (const char*)a.out : (const char*)a.ws + (m & 1) * (WS_BYTES - 64);
          };
          touch2(at(j), at(j + 32));
        }
      }
      __builtin_amdgcn_s_sleep(64);
    }
    signal_add((LAS u32*)FL + F_Q, lane);
    u32 shift = 0u;
#pragma unroll 1
    for (int t = 0; t < 4; ++t) {
      wait_flag<8>(FL + F_K + t, 4u, dead);
      if (t == 0) {
#pragma unroll 1
        for (int vw = 0; vw < 5; ++vw) pack_wo(L, vw, lane);
      }
      LAS u32x4* sp = lp<u32x4>(L, L_S + ((lane >> 4) * 4 + t) * 512 + (lane & 15) * 32);
      const u32x4 s0 = sp[0], s1 = sp[1];
      float s[16];
#pragma unroll
      for (int e = 0; e < 4; ++e) { s[2 * e] = lo_f(s0[e]); s[2 * e + 1] = hi_f(s0[e]); s[8 + 2 * e] = lo_f(s1[e]); s[8 + 2 * e + 1] = hi_f(s1[e]); }
      float m = s[0];
#pragma unroll
      for (int e = 1; e < 16; ++e) m = fmaxf(m, s[e]);
      m = wave_max(m);
      const float L2E = 1.4426950408889634f;
      float l = 0.f;
#pragma unroll
      for (int e = 0; e < 16; ++e) l += __builtin_amdgcn_exp2f((s[e] - m) * L2E);
      l = wave_sum(l);
      const u32x4 cg = lp<u32x4>(L, L_CG)[t];
      const u32 cgv = (cg.x & 0xffffu) + (cg.y & 0xffffu) + (cg.z & 0xffffu) + (cg.w & 0xffffu), cgl = max(max(cg.x >> 16, cg.y >> 16), max(cg.z >> 16, cg.w >> 16));
      put_rec(ws, lane < 2 ? WS_X1 + (u32)((t * 8 + xg) * 32 + j) * 128u + (u32)lane * 16u : OOB, lane ? cgv : __float_as_uint(m), lane ? cgl : __float_as_uint(l), T0, T1);
      u32 off[1] = {WS_X1 + (lane < 32 ? (u32)((t * 8 + xg) * 32 + lane) * 128u : (u32)((t * 8 + j8) * 32 + (r & 3) * 8 + 3) * 128u + 16u)}, d0[1], d1[1];
      __builtin_amdgcn_s_sleep(32);
      get_recs<1, 32>(ws, off, T0, T1, d0, d1, dead);
      const float mj = lane < 32 ? __uint_as_float(d0[0]) : -INFINITY, lj = lane < 32 ? __uint_as_float(d1[0]) : 0.f;
      const float M = wave_max(mj);
      {
        const float gv = wave_sum(lane < 32 ? 0.f : (float)d0[0]), gl = wave_sum(lane < 32 ? 0.f : (float)d1[0]);
        if (gv > 6.f * 2048.f || gl > 30.f * 32.f) { shift += YIELD; FL[F_SHIFT] = shift; }
      }
      float Ls = row_sum(lj * __builtin_amdgcn_exp2f((mj - M) * L2E));
      Ls = tsum16(Ls, Ls);
      Ls = __uint_as_float(__builtin_amdgcn_readfirstlane(__float_as_uint(Ls)));
      const float inv = 1.f / Ls;
      u32x4 p0, p1;
#pragma unroll
      for (int e = 0; e < 4; ++e) {
        p0[e] = pk_bf16(__builtin_amdgcn_exp2f((s[2 * e] - M) * L2E) * inv, __builtin_amdgcn_exp2f((s[2 * e + 1] - M) * L2E) * inv);
        p1[e] = pk_bf16(__builtin_amdgcn_exp2f((s[8 + 2 * e] - M) * L2E) * inv, __builtin_amdgcn_exp2f((s[8 + 2 * e + 1] - M) * L2E) * inv);
      }
      sp[0] = p0; sp[1] = p1;
      signal_add((LAS u32*)FL + F_P + t, lane);
    }
#pragma unroll 1
    for (int t = 0; t < 3; ++t) {
      wait_flag<8>(FL + F_V + t, 4u, dead);
      publish192(ws, lp<f32x2>(L, L_PART + t * 3072), WS_X2 + (u32)(((t * 8 + xg) * 32 + j) * 96) * 16u, lane, T0, T1);
      if (tj == t) {
        __builtin_amdgcn_s_sleep(72);
        {
          const int rc = lane & 15;
          u32 off[8], d0[8], d1[8];
#pragma unroll
          for (int n = 0; n < 8; ++n) off[n] = WS_X2 + (u32)(((t * 8 + xg) * 32 + 4 * n + (lane >> 4)) * 96 + 12 * c8 + (rc < 12 ? rc : 0)) * 16u;
          get_recs<8, 8>(ws, off, T0, T1, d0, d1, dead);
          float s0 = 0.f, s1 = 0.f;
#pragma unroll
          for (int n = 0; n < 8; ++n) { s0 += __uint_as_float(d0[n]); s1 += __uint_as_float(d1[n]); }
          s0 = tsum16(s0, s0); s1 = tsum16(s1, s1); s0 = tsum32(s0, s0); s1 = tsum32(s1, s1);
          const u32 op = pk_bf16(s0, s1), on = __float_as_uint(dpp<X1>(__uint_as_float(op)));
          put_rec(ws, (lane < 12 && !(lane & 1)) ? WS_XO + (u32)(xg * 24 + j) * 128u + (u32)lane * 8u : OOB, op, on, T0, T1);
        }
        __builtin_amdgcn_s_sleep(40);
        {
          const int l6 = (lane * 43) >> 8;
          u32 off[1] = {WS_XO + (lane < 48 ? (u32)(xg * 24 + 8 * t + l6) * 128u + (u32)(lane - 6 * l6) * 16u : (u32)(xg * 24 + 8 * t) * 128u)}, d0[1], d1[1];
          get_recs<1, 8>(ws, off, T0, T1, d0, d1, dead);
          if (lane < 48) lp<u32x2>(L, L_OB)[lane] = u32x2{d0[0], d1[0]};
          asm volatile("s_waitcnt lgkmcnt(0)" ::: "memory");
        }
#pragma unroll 1
        for (int vw = 0; vw < 5; ++vw) {
          u32x4 wp[6];
          load_wo(L, vw, lane, wp);
          f32x2 y = gemv16(lp<u32>(L, L_OB), wp, lane);
          if (t > 0) y += lp<f32x2>(L, L_Y)[8 * vw + j8];
          put_rec(ws, lane < 8 ? WS_XC + (u32)(xg * 24 + j) * 640u + (u32)(8 * vw + lane) * 16u : OOB, __float_as_uint(y.x), __float_as_uint(y.y), T0, T1);
        }
      }
      if (tj == t + 1) {
        __builtin_amdgcn_s_sleep(127); __builtin_amdgcn_s_sleep(127); __builtin_amdgcn_s_sleep(64);
        u32 off[1] = {WS_XC + (u32)(xg * 24 + j - 8) * 640u + (u32)(lane < 40 ? lane : 0) * 16u}, d0[1], d1[1];
        get_recs<1, 8>(ws, off, T0, T1, d0, d1, dead);
        if (lane < 40) lp<u32x2>(L, L_Y)[lane] = u32x2{d0[0], d1[0]};
      }
    }
    if (tj < 2) {
      wait_flag<8>(FL + F_BLK, 1u, dead);
      asm volatile("" :: "v"(code_touch(2 * j) | code_touch(2 * j + 1)));
      const u32 og = (u32)(40 * j + r) * (F * 2u) + 384u * xg + (u32)j8 * 16u;
      const u32 od = (lane & 15) < 12 ? (u32)(96 * j + (lane >> 4)) * (D * 2u) + ((160u * xg) & ~63u) + (u32)(lane & 15) * 16u : OOB;
      const rsrc_t rg = mk(a.wg, D * F * 2), ru = mk(a.wu, D * F * 2), rd = mk(a.wd, F * D * 2);
      u32 v[54], keep = 0u;
      wait_flag<1>(FL + F_END, 4u, dead);
#pragma unroll
      for (int n = 0; n < 30; ++n) v[n] = __builtin_amdgcn_raw_buffer_load_b32((n & 1) ? ru : rg, og + ((n >> 1) % 3) * 128, (n / 6) * 8 * (F * 2), 0);
#pragma unroll
      for (int n = 0; n < 24; ++n) v[30 + n] = __builtin_amdgcn_raw_buffer_load_b32(rd, od, n * 4 * (D * 2), 0);
      SB();
#pragma unroll
      for (int n = 0; n < 54; ++n) keep |= v[n];
      asm volatile("" :: "v"(keep));
    }
  } else {
    u32 xs[5]; u16 xr[3]; u32x4 wqv[3][3]; u32x4 ring[RING][2];
#pragma unroll
    for (int m = 0; m < 5; ++m) xs[m] = __builtin_amdgcn_raw_buffer_load_b32(rx, (lane + 64 * m) * 4, 0, 0);
    SB();
    {
      const u32 row = (u32)(80 * (j & 7) + 20 * w + r);
      const u32 o = row * (u32)(H * DH * 2) + (u32)(8 * hg(tj) + xg) * 384u + (u32)j8 * 16u;
#pragma unroll
      for (int g = 0; g < 3; ++g) {
        const u32 og = (g < 2 || r < 4) ? o : OOB, ox = (g < 2 || r < 4) ? row * 2u : OOB;
#pragma unroll
        for (int i = 0; i < 3; ++i) wqv[g][i] = __builtin_amdgcn_raw_buffer_load_b128(rq, og + i * 128, g * 8 * (H * DH * 2), AUX_NT);
        xr[g] = __builtin_amdgcn_raw_buffer_load_b16(rx, ox, g * 16, 0);
      }
    }
    ev = __builtin_amdgcn_raw_buffer_load_b64(ws, eoff, 0, AUX_SC1);
    xme = __builtin_amdgcn_raw_buffer_load_b32(rx, tid * 4, 0, 0);
    SB();
    auto issue = [&](int slot, int n, int i) {
      const rsrc_t rs = mk(n < 64 ? a.k : a.v, n < 128 ? HEADB * H : 0u);
      const u32 so = step_off(n & 63);
      ring[slot][0] = __builtin_amdgcn_raw_buffer_load_b128(rs, voff + i * 128, so, AUX_NT);
      ring[slot][1] = __builtin_amdgcn_raw_buffer_load_b128(rs, voff + 3072 + i * 128, so, AUX_NT);
      SB();
    };
    __builtin_amdgcn_s_barrier();
#pragma unroll
    for (int u = 0; u < 3; ++u) issue(u, 0, u);
    float acc[24];
    {
      float s = 0.f;
#pragma unroll
      for (int m = 0; m < 5; ++m) s += lo_f(xs[m]) + hi_f(xs[m]);
      const float mean = wave_sum(s) * (1.f / D);
      float q2 = 0.f;
#pragma unroll
      for (int m = 0; m < 5; ++m) { const float d0 = lo_f(xs[m]) - mean, d1 = hi_f(xs[m]) - mean; q2 += d0 * d0 + d1 * d1; }
      const float var = rbf(wave_sum(q2) * (1.f / D));
      const float mb = rbf(mean), inv = __builtin_amdgcn_rcpf(rbf(__builtin_amdgcn_sqrtf(rbf(var + EPS))));
#pragma unroll
      for (int e = 0; e < 24; ++e) acc[e] = 0.f;
#pragma unroll
      for (int g = 0; g < 3; ++g) {
        const u32 xl = pk_bf16(rbf(lo_f(xr[g]) - mb) * inv, 0.f), xh = xl << 16;
#pragma unroll
        for (int i = 0; i < 3; ++i)
#pragma unroll
          for (int e = 0; e < 4; ++e) {
            acc[i * 8 + 2 * e] = dot2(wqv[g][i][e], xl, acc[i * 8 + 2 * e]);
            acc[i * 8 + 2 * e + 1] = dot2(wqv[g][i][e], xh, acc[i * 8 + 2 * e + 1]);
          }
      }
      flush24(acc, lp<float>(L, L_PART + w * 768), lane);
    }
    bar();
#pragma unroll
    for (int u = 3; u < RING; ++u) issue(u, 1, u - 3);
    make_tag();
    __builtin_amdgcn_s_barrier();
    u32x4 wov[8];
    {
      const rsrc_t ro = mk(a.wo, H * DH * D * 2);
#pragma unroll
      for (int n = 0; n < 8; ++n) {
        const u32 c = (u32)(480 * w + 64 * n + lane), row = (c * 6554u) >> 16;
        const u32 oo = ((u32)(8 * hg(tj) + xg) * DH + row) * (D * 2) + 160u * c8 + (c - 10u * row) * 16u;
        wov[n] = __builtin_amdgcn_raw_buffer_load_b128(ro, (n < 7 || lane < 32) ? oo : OOB, 0, AUX_NT);
      }
    }
    SB();
    bar();
    u32 sch = (u32)__builtin_amdgcn_readfirstlane(FL[F_ANC]) * 16u + (u32)j * PACE / 32u, nxt = sch;
    u32 inflight = 0u, applied = 0u; int behind = 0;
    auto pace = [&]() {
      u32 now = (u32)rt() * 16u;
      behind = (int)(now - sch);
      while ((int)(nxt - now) > 0) { __builtin_amdgcn_s_sleep(1); now = (u32)rt() * 16u; }
      inflight += __builtin_amdgcn_s_getreg(7 | (3 << 11));
      sch += PACE; nxt = now + MINGAP;
      if ((int)(sch - nxt) > 0) nxt = sch;
    };
    auto report = [&](int t) { lp<u32>(L, L_CG)[4 * t + w] = inflight | ((u32)(behind < 0 ? 0 : behind >> 4) << 16); inflight = 0u; };
    auto follow = [&]() {
      const u32 s = __builtin_amdgcn_readfirstlane(FL[F_SHIFT]);
      sch += s - applied; applied = s;
      if ((int)(sch - nxt) > 0) nxt = sch;
    };
    LAS u32x4* sp = lp<u32x4>(L, L_S + w * 2048) + r;
    const float SCALE = 0.07216878364870322f;
    u32x4 q[3];
    auto k_block = [&](int bk, bool first) {
      u32x4 sc;
#pragma unroll
      for (int st = 0; st < 4; ++st) {
        float d[2] = {0.f, 0.f};
#pragma unroll
        for (int i = 0; i < 3; ++i) {
          const int u = 3 * st + i, slot = u % RING;
          if (i == 0) pace();
#pragma unroll
          for (int e = 0; e < 4; ++e) { d[0] = dot2(ring[slot][0][e], q[i][e], d[0]); d[1] = dot2(ring[slot][1][e], q[i][e], d[1]); }
          asm volatile("" : "+v"(d[0]), "+v"(d[1]));
          SB();
          issue(slot, 4 * bk + (u + RING) / 3, (u + RING) % 3);
        }
#pragma unroll
        for (int g = 0; g < 2; ++g) { d[g] += dpp<X1>(d[g]); d[g] += dpp<X2>(d[g]); d[g] += dpp<X7>(d[g]); }
        const u32 s1 = pk_bf16(d[0], d[1]);
        sc[st] = pk_bf16(lo_f(s1) * SCALE, hi_f(s1) * SCALE);
        if (first && st == 1) {
#pragma unroll
          for (int n = 0; n < 8; ++n) if (n < 7 || lane < 32) lp<u32x4>(L, L_WO)[480 * w + 64 * n + lane] = wov[n];
        }
      }
      if (j8 == 0) sp[bk * 8] = sc;
    };
    auto load_q = [&](int t) {
#pragma unroll
      for (int i = 0; i < 3; ++i) q[i] = lp<u32x4>(L, L_Q + t * 384)[j8 + 8 * i];
    };
    load_q(0);
    k_block(0, true);
#pragma unroll 1
    for (int bk = 1; bk < 16; ++bk) {
      if ((bk & 3) == 0) { report((bk >> 2) - 1); signal_add((LAS u32*)FL + F_K + (bk >> 2) - 1, lane); wait_flag<1>(FL + F_Q, 1u, dead); load_q(bk >> 2); }
      follow();
      k_block(bk, false);
    }
    report(3);
    signal_add((LAS u32*)FL + F_K + 3, lane);
#pragma unroll 1
    for (int bk = 0; bk < 16; ++bk) {
      if ((bk & 3) == 0) {
        wait_flag<1>(FL + F_P + (bk >> 2), 1u, dead);
#pragma unroll
        for (int e = 0; e < 24; ++e) acc[e] = 0.f;
      }
      follow();
      const u32x4 pp = sp[bk * 8];
      if (bk == 14) FL[F_BLK] = 1u;
#pragma unroll
      for (int st = 0; st < 4; ++st) {
#pragma unroll
        for (int i = 0; i < 3; ++i) {
          const int u = 3 * st + i, slot = u % RING;
          if (i == 0 && 12 * bk + u + RING < 192) pace();
#pragma unroll
          for (int e = 0; e < 4; ++e) {
            acc[i * 8 + 2 * e] = dot2(zip_lo(ring[slot][0][e], ring[slot][1][e]), pp[st], acc[i * 8 + 2 * e]);
            acc[i * 8 + 2 * e + 1] = dot2(zip_hi(ring[slot][0][e], ring[slot][1][e]), pp[st], acc[i * 8 + 2 * e + 1]);
          }
#pragma unroll
          for (int e = 0; e < 8; ++e) asm volatile("" : "+v"(acc[i * 8 + e]));
          SB();
          issue(slot, 64 + 4 * bk + (u + RING) / 3, (u + RING) % 3);
          if (u + RING == 11 && bk == 15 && lane == 0) __hip_atomic_fetch_add((LAS u32*)FL + F_END, 1u, __ATOMIC_RELAXED, __HIP_MEMORY_SCOPE_WORKGROUP);
        }
      }
      if ((bk & 3) == 3) {
        flush24(acc, lp<float>(L, L_PART + ((bk >> 2) * 4 + w) * 768), lane);
        if (bk < 15) signal_add((LAS u32*)FL + F_V + (bk >> 2), lane);
      }
    }
    auto publish_last = [&]() {
      asm volatile("s_waitcnt lgkmcnt(0)" ::: "memory");
      u32 old = 0u;
      if (lane == 0) old = __hip_atomic_fetch_add((LAS u32*)FL + F_L, 1u, __ATOMIC_RELAXED, __HIP_MEMORY_SCOPE_WORKGROUP);
      if (__builtin_amdgcn_readfirstlane(old) == 3u) {
        asm volatile("" ::: "memory");
        publish192(ws, lp<f32x2>(L, L_PART + 3 * 3072), WS_X2 + (u32)(((3 * 8 + xg) * 32 + j) * 96) * 16u, lane, T0, T1);
      }
      SB();
    };
    auto ring_done = [&]() {
#pragma unroll
      for (int u = 0; u < RING; ++u) asm volatile("" :: "v"(ring[u][0]), "v"(ring[u][1]));
      SB();
    };
    if (tj == 3) { ring_done(); publish_last(); } else { publish_last(); ring_done(); }
  }

  if (tj == 3) {
    u32x4 wp[6];
    load_wo(L, w, lane, wp);
#pragma unroll
    for (int e = 0; e < 6; ++e) asm volatile("" : "+v"(wp[e]));
    if (w != SVC) {
      __builtin_amdgcn_s_sleep(12);
      float s[3];
      gather8<0>(ws, L, WS_X2 + (u32)(((3 * 8 + xg) * 32 + 8 * w) * 96) * 16u, lane, T0, T1, s);
#pragma unroll
      for (int pr = 0; pr < 3; ++pr) lp<float>(L, L_RED)[((w * 3 + pr) * 32 + (lane & 31)) * 2 + (lane >> 5)] = s[pr];
    }
    bar();
    if (w == SVC) put_rec(ws, lane < 8 ? WS_XH + (u32)(lane * 64 + xg * 8 + c8) * 128u : OOB, 0u, 0u, T0, T1);
    f32x2 y = lp<f32x2>(L, L_Y)[8 * w + j8];
    finish_o(L, lp<u32>(L, L_OB + w * 384), lane);
    y += gemv16(lp<u32>(L, L_OB + w * 384), wp, lane);
    put_rec(ws, WS_X3 + (u32)r * X3_REP + (u32)(xg * 320 + 40 * c8 + 8 * w + j8) * 16u, __float_as_uint(y.x), __float_as_uint(y.y), T0, T1);
    SB();
    __builtin_amdgcn_s_barrier();
    SB();
    if (w < 3) {
      u32x4 da[8]; u32 db[8];
      const rsrc_t rd = mk(a.wd, F * D * 2);
      const u32 od = (u32)tid * (8u * D * 2u) + 160u * xg + 20u * c8;
#pragma unroll
      for (int n = 0; n < 8; ++n) {
        da[n] = __builtin_amdgcn_raw_buffer_load_b128(rd, od, n * D * 2, 0);
        db[n] = __builtin_amdgcn_raw_buffer_load_b32(rd, od + 16u, n * D * 2, 0);
      }
      u32 dp[4][10];
#pragma unroll
      for (int n = 0; n < 4; ++n) {
#pragma unroll
        for (int e = 0; e < 4; ++e) { dp[n][2 * e] = zip_lo(da[2 * n][e], da[2 * n + 1][e]); dp[n][2 * e + 1] = zip_hi(da[2 * n][e], da[2 * n + 1][e]); }
        dp[n][8] = zip_lo(db[2 * n], db[2 * n + 1]); dp[n][9] = zip_hi(db[2 * n], db[2 * n + 1]);
#pragma unroll
        for (int e = 0; e < 10; ++e) asm volatile("" : "+v"(dp[n][e]));
      }
      float c[10];
#pragma unroll
      for (int e = 0; e < 10; ++e) { c[e] = 0.f; asm volatile("" : "+v"(c[e])); }
      u32 off[2] = {WS_X4 + (u32)xg * X4_REP + (u32)tid * 128u, WS_X4 + (u32)xg * X4_REP + (u32)tid * 128u + 16u}, d0[2], d1[2];
      get_recs<2, 2>(ws, off, T0, T1, d0, d1, dead);
      float c5[5];
#pragma unroll
      for (int e = 0; e < 10; ++e) c[e] = dot2(dp[3][e], d1[1], dot2(dp[2][e], d0[1], dot2(dp[1][e], d1[0], dot2(dp[0][e], d0[0], c[e]))));
#pragma unroll
      for (int e = 0; e < 5; ++e) c5[e] = tsum32(c[e], c[e + 5]);
      const float v0 = row_sum(tsum16(c5[0], c5[1])), v1 = row_sum(tsum16(c5[2], c5[3])), v2 = row_sum(tsum16(c5[4], c5[4]));
      if ((lane & 15) == 0) {
        LAS float* dn = lp<float>(L, L_DN) + w * 16 + 5 * (lane >> 5);
        const int b4 = (lane >> 4) & 1;
        dn[b4] = v0; dn[2 + b4] = v1; dn[4] = v2;
      }
    } else if (w == 3) {
      const int cp = lane & 7;
      u32 off[1] = {WS_X3 + (u32)xg * X3_REP + (u32)(r * 320 + 40 * xg + 5 * c8 + (cp < 5 ? cp : 0)) * 16u}, d0[1], d1[1];
      const u32 xv = __builtin_amdgcn_raw_buffer_load_b32(rx, (40 * xg + 5 * c8 + (cp < 5 ? cp : 0)) * 4, 0, 0);
      __builtin_amdgcn_s_sleep(12);
      get_recs<1, 8>(ws, off, T0, T1, d0, d1, dead);
      float s0 = __uint_as_float(d0[0]), s1 = __uint_as_float(d1[0]);
      s0 = tsum32(s0, s0); s1 = tsum32(s1, s1); s0 = tsum16(s0, s0); s1 = tsum16(s1, s1); s0 += dpp<R8>(s0); s1 += dpp<R8>(s1);
      const u32 at = pk_bf16(s0, s1);
      if (lane < 5) lp<u32>(L, L_H)[lane] = pk_bf16(lo_f(xv) + lo_f(at), hi_f(xv) + hi_f(at));
    }
    bar();
    if (tid < 10) {
      const float s = rbf((lp<float>(L, L_DN)[tid] + lp<float>(L, L_DN)[16 + tid]) + lp<float>(L, L_DN)[32 + tid]);
      a.out[80 * xg + 10 * c8 + tid] = (u16)pk_bf16(lo_f(lp<u16>(L, L_H)[tid]) + s, 0.f);
    }
  } else {
    bar();
    u32 gp[16];
    {
      const rsrc_t rg = mk(a.wg, D * F * 2), ru = mk(a.wu, D * F * 2);
      const u32 og = (u32)tid * (2u * F * 2u) + 384u * xg + 16u * j;
      const u32x4 A = __builtin_amdgcn_raw_buffer_load_b128(rg, og, 0, 0), B = __builtin_amdgcn_raw_buffer_load_b128(rg, og, F * 2, 0);
      const u32x4 C = __builtin_amdgcn_raw_buffer_load_b128(ru, og, 0, 0), E = __builtin_amdgcn_raw_buffer_load_b128(ru, og, F * 2, 0);
#pragma unroll
      for (int e = 0; e < 4; ++e) {
        gp[2 * e] = zip_lo(A[e], B[e]); gp[2 * e + 1] = zip_hi(A[e], B[e]);
        gp[8 + 2 * e] = zip_lo(C[e], E[e]); gp[8 + 2 * e + 1] = zip_hi(C[e], E[e]);
      }
#pragma unroll
      for (int e = 0; e < 16; ++e) asm volatile("" : "+v"(gp[e]));
    }
    if (w == SVC) {
      u32 off[1] = {WS_XH + (u32)(xg * 64 + lane) * 128u}, d0[1], d1[1];
      get_recs<1, 2>(ws, off, T0, T1, d0, d1, dead);
    }
    bar();
    u32 hp;
    {
      u32 off[8], d0[8], d1[8];
#pragma unroll
      for (int k = 0; k < 8; ++k) off[k] = WS_X3 + (u32)xg * X3_REP + (u32)(k * 320 + tid) * 16u;
      get_recs<8, 0>(ws, off, T0, T1, d0, d1, dead);
      auto sum8 = [](const u32 (&d)[8]) {
        float t[4];
#pragma unroll
        for (int k = 0; k < 4; ++k) t[k] = __uint_as_float(d[k]) + __uint_as_float(d[k + 4]);
        return (t[0] + t[2]) + (t[1] + t[3]);
      };
      const u32 at = pk_bf16(sum8(d0), sum8(d1));
      hp = pk_bf16(lo_f(xme) + lo_f(at), hi_f(xme) + hi_f(at));
    }
    u32 x4o = (lane & 3) == 0 ? WS_X4 + (u32)r * X4_REP + (u32)(xg * 24 + j) * 128u + (u32)(lane & 4) * 4u : OOB;
    const float h0 = lo_f(hp), h1 = hi_f(hp);
    {
      const float c = __uint_as_float(__builtin_amdgcn_readfirstlane(__float_as_uint(h0)));
      const float e0 = h0 - c, e1 = h1 - c;
      const float s = row_sum(tsum16(e0 + e1, e0 * e0 + e1 * e1));
      const float t = tsum32(s, s);
      auto p = __builtin_amdgcn_permlane16_swap(__float_as_uint(t), __float_as_uint(t), false, false);
      const float s1 = __uint_as_float(p[0]) * (1.f / 128.f);
      if (lane == 0) { lp<float>(L, L_ST)[w] = c + s1; lp<float>(L, L_ST)[8 + w] = __uint_as_float(p[1]) - 128.f * s1 * s1; }
    }
    float g[16];
#pragma unroll
    for (int e = 0; e < 16; ++e) { g[e] = 0.f; asm volatile("" : "+v"(g[e])); }
    asm volatile("" : "+v"(x4o));
    bar();
    const f32x4 mw = lp<f32x4>(L, L_ST)[0], qw = lp<f32x4>(L, L_ST)[2];
    const float mw4 = lp<float>(L, L_ST)[4], qw4 = lp<float>(L, L_ST)[12];
    const float mean = (((mw.x + mw.z) + (mw.y + mw.w)) + mw4) * 0.2f;
    const f32x4 dm = mw - mean; const float d4 = mw4 - mean;
    const float m2 = (((qw.x + qw.z) + (qw.y + qw.w)) + qw4) + 128.f * (((dm.x * dm.x + dm.z * dm.z) + (dm.y * dm.y + dm.w * dm.w)) + d4 * d4);
    const float var = rbf(m2 * (1.f / D));
    const float mb = rbf(mean), inv = __builtin_amdgcn_rcpf(rbf(__builtin_amdgcn_sqrtf(rbf(var + EPS))));
    const u32 dd = pk_bf16(h0 - mb, h1 - mb);
    const u32 hn = pk_bf16(lo_f(dd) * inv, hi_f(dd) * inv);
#pragma unroll
    for (int e = 0; e < 16; ++e) g[e] = dot2(gp[e], hn, g[e]);
    float g8[8], g4[4], g2[2];
#pragma unroll
    for (int e = 0; e < 8; ++e) g8[e] = tsum32(g[e], g[e + 8]);
#pragma unroll
    for (int e = 0; e < 4; ++e) g4[e] = tsum16(g8[e], g8[e + 4]);
    const bool b3 = (lane & 8) != 0, b2 = (lane & 4) != 0;
#pragma unroll
    for (int e = 0; e < 2; ++e) g2[e] = (b3 ? g4[e + 2] : g4[e]) + dpp<R8>(b3 ? g4[e] : g4[e + 2]);
    float g1 = (b2 ? g2[1] : g2[0]) + dpp<X7>(b2 ? g2[0] : g2[1]);
    g1 += dpp<X2>(g1); g1 += dpp<X1>(g1);
    if ((lane & 3) == 0) lp<float>(L, L_GU)[w * 16 + (lane >> 2)] = g1;
    bar();
    if (b == 0 && tid == 64) {
      const u64 e1 = (((u64)ev.y << 32) | ev.x) + 1ull;
      __builtin_amdgcn_raw_buffer_store_b64(u32x2{(u32)e1, (u32)(e1 >> 32)}, ws, WS_E, 0, AUX_SC1);
    }
    if (w == 0) {
      float gs = 0.f, us = 0.f;
#pragma unroll
      for (int ww = 0; ww < 5; ++ww) { gs += lp<float>(L, L_GU)[ww * 16 + j8]; us += lp<float>(L, L_GU)[ww * 16 + 8 + j8]; }
      const u32 gu = pk_bf16(gs, us);
      const float sg = rbf(__builtin_amdgcn_rcpf(1.f + __builtin_amdgcn_exp2f(-1.4426950408889634f * lo_f(gu))));
      const float f = sg * hi_f(gu);
      const u32 d0 = pk_bf16(dpp<0x00>(f), dpp<0x55>(f)), d1 = pk_bf16(dpp<0xAA>(f), dpp<0xFF>(f));
      put_rec(ws, x4o, d0, d1, T0, T1);
    }
  }
  asm volatile(".Lfb_end:");
}
}

extern "C" void kernel_launch(void* const* d_in, const int* in_sizes, int n_in, void* d_out, int out_size, void* d_ws, size_t ws_size,
                              hipStream_t stream) {
  Args a;
  a.x = (const u16*)d_in[0]; a.k = (const u16*)d_in[1]; a.v = (const u16*)d_in[2]; a.wq = (const u16*)d_in[3]; a.wo = (const u16*)d_in[4];
  a.wg = (const u16*)d_in[6]; a.wu = (const u16*)d_in[7]; a.wd = (const u16*)d_in[8];
  a.out = (u16*)d_out; a.ws = (unsigned char*)d_ws;
  hipLaunchKernelGGL(fused_block, dim3(256), dim3(NTHR), 0, stream, a);
}
```
